```python
import torch

def _softmax(x, axis=-1):
    return torch.softmax(x, dim=axis)

def _gelu(x):
    return torch.nn.functional.gelu(x, approximate='tanh')

def _silu(x):
    return torch.nn.functional.silu(x)

def _sigmoid(x):
    return torch.sigmoid(x)

def _layernorm(x, axis=-1, eps=1e-05):
    m = x.mean(dim=axis, keepdim=True)
    v = x.var(dim=axis, keepdim=True, unbiased=False)
    return (x - m) / torch.sqrt(v + eps)

def _scatter_add(tgt, idx, src, axis=0):
    return tgt.clone().index_add_(axis, idx, src)

def _segment_sum(src, seg, n):
    out = torch.zeros((n,) + src.shape[1:], dtype=src.dtype)
    return out.index_add_(0, seg, src)

def _pad(x, pw):
    flat = tuple((v for pair in reversed(pw) for v in pair))
    return torch.nn.functional.pad(x, flat)
kernel_name = 'adaLN_cond__gqa_block_cached__dense_activation_squared_relu__decode'

def setup_inputs():

    _w = lambda a: (lambda t: t.to(torch.bfloat16) if t.is_floating_point() else t)(torch.as_tensor(a))

    def _A_setup():
        B, Sq, D = (2, 1, 576)
        Hq, Hkv, Sk, Dh = (32, 16, 8192, 128)
        assert Hq % Hkv == 0, (Hq, Hkv)
        return {'x': _w((lambda *s: torch.randn(*s, dtype=torch.bfloat16) * 0.05)(B, Sq, D)), 'k_cache': _w((lambda *s: torch.randn(*s, dtype=torch.bfloat16) * 3.2)(B, Hkv, Sk, Dh)), 'v_cache': _w((lambda *s: torch.randn(*s, dtype=torch.bfloat16) * 0.4)(B, Hkv, Sk, Dh)), 'wq': _w((lambda *s: torch.randn(*s, dtype=torch.bfloat16) * 3.2)(D, Hq * Dh) * D ** (-0.5)), 'wo': _w((lambda *s: torch.randn(*s, dtype=torch.bfloat16) * 0.4)(Hq * Dh, D) * (Hq * Dh) ** (-0.5))}
    _A_in = _A_setup()

    def _B_setup():
        B, S, D, F = (2, 1, 576, 4608)
        return {'x': _w((lambda *s: torch.randn(*s, dtype=torch.bfloat16) * 0.05)(B, S, D)), 'w1': _w((lambda *s: torch.randn(*s, dtype=torch.bfloat16) * 0.05)(D, F)), 'b1': _w((lambda *s: torch.randn(*s, dtype=torch.bfloat16) * 0.4)(F)), 'w2': _w((lambda *s: torch.randn(*s, dtype=torch.bfloat16) * 0.05)(F, D)), 'b2': _w((lambda *s: torch.randn(*s, dtype=torch.bfloat16) * 3.2)(D))}
    _B_in = _B_setup()
    return {'A_x': _A_in['x'], 'A_k_cache': _A_in['k_cache'], 'A_v_cache': _A_in['v_cache'], 'A_wq': _A_in['wq'], 'A_wo': _A_in['wo'], 'B_x': _B_in['x'], 'B_w1': _B_in['w1'], 'B_b1': _B_in['b1'], 'B_w2': _B_in['w2'], 'B_b2': _B_in['b2'], 'c': _w((lambda *s: torch.randn(*s, dtype=torch.bfloat16) * 0.1)(2, 576) * 8.0), 'w_mod': _w((lambda *s: torch.randn(*s, dtype=torch.bfloat16) * 0.1)(576, 6 * 576) * 576 ** (-0.5) * 8.0)}

def reference(A_x, A_k_cache, A_v_cache, A_wq, A_wo, B_x, B_w1, B_b1, B_w2, B_b2, c, w_mod):

    def _A_ref(x, k_cache, v_cache, wq, wo):
        B, Sq, D = x.shape
        Hq, Dh = (32, 128)
        Hkv = k_cache.shape[1]
        rep = Hq // Hkv
        q = (lambda x, *d: x.permute(*d))((x @ wq).reshape(B, Sq, Hq, Dh), 0, 2, 1, 3)
        k = (lambda x, n, axis: torch.repeat_interleave(x, n, dim=axis))(k_cache, rep, axis=1)
        v = (lambda x, n, axis: torch.repeat_interleave(x, n, dim=axis))(v_cache, rep, axis=1)
        scores = torch.einsum('bhqd,bhkd->bhqk', q, k) * Dh ** (-0.5)
        p = _softmax(scores, axis=-1)
        o = torch.einsum('bhqk,bhkd->bhqd', p, v)
        return (lambda x, *d: x.permute(*d))(o, 0, 2, 1, 3).reshape(B, Sq, Hq * Dh) @ wo

    def _B_ref(x, w1, b1, w2, b2):
        h = torch.einsum('bsd,df->bsf', x, w1) + b1
        zero = torch.zeros_like(h)
        r = torch.where(h > zero, h, zero)
        h = r * r
        out = torch.einsum('bsf,fd->bsd', h, w2) + b2
        return out
    x = A_x
    mod = (_silu(c) @ w_mod).reshape(c.shape[0], 6, 1, -1)
    sa, ca, ga, sb, cb, gb = (mod[:, 0].clone(), mod[:, 1].clone(), mod[:, 2].clone(), mod[:, 3].clone(), mod[:, 4].clone(), mod[:, 5].clone())
    h = _layernorm(x, axis=-1) * (1 + ca) + sa
    x = x + ga * _A_ref(h, A_k_cache, A_v_cache, A_wq, A_wo)
    h = _layernorm(x, axis=-1) * (1 + cb) + sb
    x = x + gb * _B_ref(h, B_w1, B_b1, B_w2, B_b2)
    return x
```

```cpp
#include <hip/hip_runtime.h>
#include <hip/hip_bf16.h>
#include <stdint.h>
#define MIMIC 1
typedef unsigned short u16;
namespace base {
constexpr int B = 2, D = 576, HQ = 32, HKV = 16, SK = 8192, DH = 128, F = 4608;
__device__ __forceinline__ float bf2f(u16 v) { return __uint_as_float((unsigned)v << 16); }
__device__ __forceinline__ u16 f2bf(float f) { unsigned u = __float_as_uint(f); u += 0x7fffu + ((u >> 16) & 1u); return (u16)(u >> 16); }
__device__ __forceinline__ float rb(float f) { return MIMIC ? bf2f(f2bf(f)) : f; }

__global__ void gemv(const float* x, const u16* w, float* y, int K, int N) {
  __shared__ float red[8][64][2];
  int n = blockIdx.x * 64 + threadIdx.x, ks = threadIdx.y;
  float a0 = 0, a1 = 0;
  for (int k = ks; k < K; k += 8) { float wv = bf2f(w[(size_t)k * N + n]); a0 += x[k] * wv; a1 += x[K + k] * wv; }
  red[ks][threadIdx.x][0] = a0; red[ks][threadIdx.x][1] = a1;
  __syncthreads();
  if (ks == 0) { float s0 = 0, s1 = 0; for (int i = 0; i < 8; ++i) { s0 += red[i][threadIdx.x][0]; s1 += red[i][threadIdx.x][1]; }
    y[n] = rb(s0); y[N + n] = rb(s1); }
}
__global__ void silu_k(const u16* c, float* o) { int i = blockIdx.x * 64 + threadIdx.x; float v = bf2f(c[i]); o[i] = rb(v / (1.f + expf(-v))); }
__global__ void ln_k(const float* x, const float* mod, int so, int co, float* h) {
  int b = blockIdx.x, t = threadIdx.x; __shared__ float s[D];
  float v = x[b * D + t]; s[t] = v; __syncthreads();
  float m = 0; for (int i = 0; i < D; ++i) m += s[i]; m /= D;
  float var = 0; for (int i = 0; i < D; ++i) var += (s[i] - m) * (s[i] - m); var /= D;
  float mb = rb(m), vb = rb(var);
  float den = rb(sqrtf(rb(vb + 1e-5f)));
  float n = rb(rb(v - mb) / den);
  h[b * D + t] = rb(rb(n * rb(1.f + mod[b * 6 * D + co * D + t])) + mod[b * 6 * D + so * D + t]);
}
__global__ void cvt_k(const u16* x, float* o) { int i = blockIdx.x * 64 + threadIdx.x; o[i] = bf2f(x[i]); }
__global__ void score_k(const float* q, const u16* k, float* s) {
  int key = blockIdx.x * 64 + threadIdx.x, h = blockIdx.y, b = blockIdx.z;
  const u16* kr = k + (((size_t)b * HKV + h / 2) * SK + key) * DH; const float* qr = q + (b * HQ + h) * DH;
  float a = 0; for (int d = 0; d < DH; ++d) a += qr[d] * bf2f(kr[d]);
  s[((size_t)b * HQ + h) * SK + key] = rb(rb(a) * 0.08838834764831845f);
}
__global__ void softmax_k(float* s) {
  float* r = s + (size_t)blockIdx.x * SK; __shared__ float red[256]; int t = threadIdx.x;
  float m = -INFINITY; for (int i = t; i < SK; i += 256) m = fmaxf(m, r[i]);
  red[t] = m; __syncthreads(); for (int o = 128; o; o >>= 1) { if (t < o) red[t] = fmaxf(red[t], red[t + o]); __syncthreads(); }
  m = red[0]; __syncthreads();
  float l = 0; for (int i = t; i < SK; i += 256) l += expf(r[i] - m);
  red[t] = l; __syncthreads(); for (int o = 128; o; o >>= 1) { if (t < o) red[t] += red[t + o]; __syncthreads(); }
  l = red[0];
  for (int i = t; i < SK; i += 256) r[i] = rb(expf(r[i] - m) / l);
}
__global__ void pv_k(const float* p, const u16* v, float* o) {
  int h = blockIdx.x, b = blockIdx.y, d = threadIdx.x, ks = threadIdx.y; __shared__ float red[8][128];
  const u16* vr = v + ((size_t)b * HKV + h / 2) * SK * DH; const float* pr = p + ((size_t)b * HQ + h) * SK;
  float a = 0; for (int k = ks; k < SK; k += 8) a += pr[k] * bf2f(vr[(size_t)k * DH + d]);
  red[ks][d] = a; __syncthreads();
  if (ks == 0) { float s = 0; for (int i = 0; i < 8; ++i) s += red[i][d]; o[(b * HQ + h) * DH + d] = rb(s); }
}
__global__ void res_k(const float* x, const float* mod, int go, const float* a, const u16* bias, float* y, u16* out) {
  int b = blockIdx.x, t = threadIdx.x; float av = a[b * D + t]; if (bias) av = rb(av + bf2f(bias[t]));
  float r = rb(x[b * D + t] + rb(mod[b * 6 * D + go * D + t] * av));
  if (y) y[b * D + t] = r; if (out) out[b * D + t] = f2bf(r);
}
__global__ void act_k(float* h, const u16* b1) { int i = blockIdx.x * 64 + threadIdx.x; float v = rb(h[i] + bf2f(b1[i % F])); v = v > 0 ? v : 0; h[i] = rb(v * v); }

static void run(void* const* d_in, void* d_out, void* d_ws, hipStream_t st) {
  const u16 *x = (const u16*)d_in[0], *kc = (const u16*)d_in[1], *vc = (const u16*)d_in[2], *wq = (const u16*)d_in[3], *wo = (const u16*)d_in[4];
  const u16 *w1 = (const u16*)d_in[6], *b1 = (const u16*)d_in[7], *w2 = (const u16*)d_in[8], *b2 = (const u16*)d_in[9], *c = (const u16*)d_in[10], *wm = (const u16*)d_in[11];
  float* ws = (float*)d_ws;
  float *sc = ws, *mod = ws + 2048, *xf = ws + 16384, *h = ws + 18432, *q = ws + 20480, *o = ws + 32768, *at = ws + 49152, *x1 = ws + 51200, *h2 = ws + 53248, *h1 = ws + 55296, *mo = ws + 69632, *s = ws + 131072;
  silu_k<<<18, 64, 0, st>>>(c, sc);
  gemv<<<6 * D / 64, dim3(64, 8), 0, st>>>(sc, wm, mod, D, 6 * D);
  cvt_k<<<18, 64, 0, st>>>(x, xf);
  ln_k<<<2, D, 0, st>>>(xf, mod, 0, 1, h);
  gemv<<<HQ * DH / 64, dim3(64, 8), 0, st>>>(h, wq, q, D, HQ * DH);
  score_k<<<dim3(SK / 64, HQ, B), 64, 0, st>>>(q, kc, s);
  softmax_k<<<B * HQ, 256, 0, st>>>(s);
  pv_k<<<dim3(HQ, B), dim3(128, 8), 0, st>>>(s, vc, o);
  gemv<<<D / 64, dim3(64, 8), 0, st>>>(o, wo, at, HQ * DH, D);
  res_k<<<2, D, 0, st>>>(xf, mod, 2, at, nullptr, x1, nullptr);
  ln_k<<<2, D, 0, st>>>(x1, mod, 3, 4, h2);
  gemv<<<F / 64, dim3(64, 8), 0, st>>>(h2, w1, h1, D, F);
  act_k<<<2 * F / 64, 64, 0, st>>>(h1, b1);
  gemv<<<D / 64, dim3(64, 8), 0, st>>>(h1, w2, mo, F, D);
  res_k<<<2, D, 0, st>>>(x1, mod, 5, mo, b2, nullptr, (u16*)d_out);
}
}
extern "C" void kernel_launch(void* const* d_in, const int* in_sizes, int n_in, void* d_out, int out_size, void* d_ws, size_t ws_size, hipStream_t stream) {
  base::run(d_in, d_out, d_ws, stream);
}
```

```cpp
#define TSG 5500
#define TQP 8500
#define TH 10
#define TV 10
#define T1 2800
#define TAU 140
#include <hip/hip_runtime.h>
#include <hip/hip_bf16.h>
#include <stdint.h>

namespace mk {
typedef unsigned short u16;
typedef unsigned v4u __attribute__((ext_vector_type(4)));
typedef unsigned v2u __attribute__((ext_vector_type(2)));
typedef float f32x4 __attribute__((ext_vector_type(4)));
typedef float f32x2 __attribute__((ext_vector_type(2)));
typedef short s16x4 __attribute__((ext_vector_type(4)));
typedef __bf16 bf16x2 __attribute__((ext_vector_type(2)));
typedef __bf16 bf16x8 __attribute__((ext_vector_type(8)));
typedef __amdgpu_buffer_rsrc_t rsrc_t;
#define LAS __attribute__((address_space(3)))
#define GAS __attribute__((address_space(1)))

constexpr int D = 576, F = 4608, SK = 8192, DH = 128, HKV = 16, NQ = 4096;
constexpr int NTHR = 512;
constexpr int AUX_NT = 2, AUX_SC1 = 16;
#define RING 56
#define PSTEP 2
#define TRIGW last_issue()
#define NL 14
#define TMS 0
#define TQS 0
#define TMG 14000
#define CLRPOS 0
#define WSOFF 0
#define S1 8
#define TRIG last_issue()
#define LTH 32
#define LTHW 32
#define H3SLEEP 12
#define KE 4
#define TWE 0
#define TWM 0
constexpr unsigned SENT = 0xffffffffu;
constexpr int SPIN = 1 << 16;

constexpr int WS_EPOCH = 0;
constexpr int WS_SG = 256;
constexpr int WS_T0 = WS_SG + 9216;
constexpr int WS_TQ = WS_T0 + 128;
constexpr int WS_QG = 16384;
constexpr int WS_MOD = WS_QG + 32768;
constexpr int WS_H2 = WS_MOD + 9216;
constexpr int WS_ML = WS_H2 + 36864;
constexpr int WS_H1 = WS_ML + 4096;
constexpr int WS_H3 = WS_H1 + 262144;
constexpr int WS_END = WS_H3 + 144 * 4608;
constexpr int WS_DBG = 4 << 20;

constexpr int L_WT = 0;
constexpr int L_W2 = L_WT + 36864;
constexpr int L_PK = L_WT + 73728;
constexpr int L_ACT = L_PK + NL * 4096;
constexpr int L_Q = L_ACT + 2304;
constexpr int L_MRG = L_Q + 512;
constexpr int L_WML = L_MRG + 4096;
constexpr int L_FLG = L_WML + 64;
constexpr int L_MOD = L_FLG + 128;
constexpr int L_X = L_MOD + 9216;
constexpr int L_X1 = L_X + 2304;
constexpr int L_H2 = L_X1 + 4608;
constexpr int L_O = L_H2 + 2560;
constexpr int L_H1 = L_O + 2048;
constexpr int L_BI = L_H1 + 128 + 16;
constexpr int L_END = L_BI + 80;
static_assert(L_END <= 160 * 1024, "LDS");
enum { F_Q = 0, F_RDY = 1, F_ACT0 = 2, F_ACT1 = 3, F_ML0 = 4  , F_V0 = 8  , F_W6 = 12, F_W7 = 13, F_D0 = 14, F_D1 = 15, F_T0 = 16, F_T0Q = 17, F_S0 = 20   };

__device__ __forceinline__ float bf_lo(unsigned u) { return __uint_as_float(u << 16); }
__device__ __forceinline__ float bf_hi(unsigned u) { return __uint_as_float(u & 0xffff0000u); }
__device__ __forceinline__ unsigned pk(float a, float b) { f32x2 v = {a, b}; return __builtin_bit_cast(unsigned, __builtin_convertvector(v, bf16x2)); }
__device__ __forceinline__ float rb(float a) { return bf_lo(pk(a, a)); }
__device__ __forceinline__ unsigned nos(unsigned u) { return u < 0xfffffffeu ? u : 0xfffffffeu; }
__device__ __forceinline__ v4u nos4(f32x4 v) { return v4u{nos(__float_as_uint(v[0])), nos(__float_as_uint(v[1])), nos(__float_as_uint(v[2])), nos(__float_as_uint(v[3]))}; }
__device__ __forceinline__ f32x4 asf(v4u v) { return __builtin_bit_cast(f32x4, v); }
__device__ __forceinline__ rsrc_t mkrs(const void* p, unsigned bytes) { return __builtin_amdgcn_make_buffer_rsrc((void*)p, 0, (int)bytes, 0x00020000); }
template <int AUX> __device__ __forceinline__ v4u ld128(rsrc_t r, int v, int s) { return __builtin_amdgcn_raw_buffer_load_b128(r, v, s, AUX); }
template <int AUX> __device__ __forceinline__ v2u ld64(rsrc_t r, int v, int s) { return __builtin_amdgcn_raw_buffer_load_b64(r, v, s, AUX); }
template <int AUX> __device__ __forceinline__ unsigned ld32(rsrc_t r, int v, int s) { return __builtin_amdgcn_raw_buffer_load_b32(r, v, s, AUX); }
template <int AUX> __device__ __forceinline__ unsigned ld16(rsrc_t r, int v, int s) { return __builtin_amdgcn_raw_buffer_load_b16(r, v, s, AUX); }
__device__ __forceinline__ void st128(rsrc_t r, int v, v4u d) { __builtin_amdgcn_raw_buffer_store_b128(d, r, v, 0, AUX_SC1); }
__device__ __forceinline__ void st64(rsrc_t r, int v, v2u d) { __builtin_amdgcn_raw_buffer_store_b64(d, r, v, 0, AUX_SC1); }
__device__ __forceinline__ void dma(rsrc_t r, LAS unsigned char* l, int v, int s) { __builtin_amdgcn_raw_ptr_buffer_load_lds(r, (LAS void*)l, 16, v, s, 0, AUX_NT); }
__device__ __forceinline__ f32x4 mfma(v4u a, v4u b, f32x4 c) { return __builtin_amdgcn_mfma_f32_16x16x32_bf16(__builtin_bit_cast(bf16x8, a), __builtin_bit_cast(bf16x8, b), c, 0, 0, 0); }
__device__ __forceinline__ v2u trd(LAS unsigned char* p) { return __builtin_bit_cast(v2u, __builtin_amdgcn_ds_read_tr16_b64_v4i16((LAS s16x4*)p)); }
__device__ __forceinline__ v4u trd2(LAS unsigned char* p0, LAS unsigned char* p1) { v2u a = trd(p0), b = trd(p1); return v4u{a.x, a.y, b.x, b.y}; }
template <int C> __device__ __forceinline__ float dppf(float x) { return __builtin_bit_cast(float, __builtin_amdgcn_mov_dpp(__builtin_bit_cast(int, x), C, 0xf, 0xf, true)); }
template <int C> __device__ __forceinline__ unsigned dppu(unsigned x) { return (unsigned)__builtin_amdgcn_mov_dpp((int)x, C, 0xf, 0xf, true); }
constexpr int QX1 = 0xB1, QX2 = 0x4E, HMIR = 0x141, ROR4 = 0x124, ROR8 = 0x128;
__device__ __forceinline__ float x16_sum(float x) { auto s = __builtin_amdgcn_permlane16_swap(__float_as_uint(x), __float_as_uint(x), false, false); return __uint_as_float(s[0]) + __uint_as_float(s[1]); }
__device__ __forceinline__ float x32_sum(float x) { auto s = __builtin_amdgcn_permlane32_swap(__float_as_uint(x), __float_as_uint(x), false, false); return __uint_as_float(s[0]) + __uint_as_float(s[1]); }
__device__ __forceinline__ float x16_max(float x) { auto s = __builtin_amdgcn_permlane16_swap(__float_as_uint(x), __float_as_uint(x), false, false); return fmaxf(__uint_as_float(s[0]), __uint_as_float(s[1])); }
__device__ __forceinline__ float x32_max(float x) { auto s = __builtin_amdgcn_permlane32_swap(__float_as_uint(x), __float_as_uint(x), false, false); return fmaxf(__uint_as_float(s[0]), __uint_as_float(s[1])); }
__device__ __forceinline__ float sum8(float x) { x += dppf<QX1>(x); x += dppf<QX2>(x); x += dppf<HMIR>(x); return x; }
__device__ __forceinline__ float max8(float x) { x = fmaxf(x, dppf<QX1>(x)); x = fmaxf(x, dppf<QX2>(x)); x = fmaxf(x, dppf<HMIR>(x)); return x; }
__device__ __forceinline__ float sum64(float x) { x = sum8(x); x += dppf<ROR8>(x); return x32_sum(x16_sum(x)); }
__device__ __forceinline__ bool okv(v4u v) { return (v.x != SENT) & (v.y != SENT) & (v.z != SENT) & (v.w != SENT); }
#define LGKM0() asm volatile("s_waitcnt lgkmcnt(0)" ::: "memory")
#define VM0() asm volatile("s_waitcnt vmcnt(0)" ::: "memory")
#define VMN(n) asm volatile("s_waitcnt vmcnt(%0)" :: "n"(n) : "memory")
#define BAR() do { asm volatile("s_waitcnt lgkmcnt(0)" ::: "memory"); __builtin_amdgcn_s_barrier(); asm volatile("" ::: "memory"); } while (0)
#define SB() __builtin_amdgcn_sched_barrier(0)
__device__ __forceinline__ void set_flag(LAS unsigned char* L, int f) { LGKM0(); *(volatile LAS unsigned*)(L + L_FLG + 4 * f) = 1u; }
__device__ __forceinline__ void wait_flag(LAS unsigned char* L, int f) { while (__builtin_amdgcn_readfirstlane(*(volatile LAS unsigned*)(L + L_FLG + 4 * f)) == 0u) __builtin_amdgcn_s_sleep(1); asm volatile("" ::: "memory"); }
__device__ __forceinline__ void wait_flag4(LAS unsigned char* L, int f) {
  for (;;) { v4u v = *(volatile LAS v4u*)(L + L_FLG + 4 * f); if (__builtin_amdgcn_readfirstlane(v.x & v.y & v.z & v.w)) break; __builtin_amdgcn_s_sleep(1); }
  asm volatile("" ::: "memory");
}
#define PIN(x) asm volatile("" : "+v"(x))
__device__ __forceinline__ f32x2 unpk(unsigned u) { return f32x2{bf_lo(u), bf_hi(u)}; }
__device__ __forceinline__ f32x2 rb2(f32x2 v) { return unpk(pk(v.x, v.y)); }
__device__ __forceinline__ unsigned mx4(v4u v) { return max(max(v.x, v.y), max(v.z, v.w)); }
template <int N> __device__ __forceinline__ f32x4 sumv(const v4u (&v)[N]) {
  if constexpr (N == 8) return ((asf(v[0]) + asf(v[1])) + (asf(v[2]) + asf(v[3]))) + ((asf(v[4]) + asf(v[5])) + (asf(v[6]) + asf(v[7])));
  else return ((asf(v[0]) + asf(v[1])) + (asf(v[2]) + asf(v[3]))) + asf(v[4]);
}
template <int N, int STR> __device__ __forceinline__ f32x4 gather(rsrc_t r, int vo) {
  v4u v[N];
#pragma unroll
  for (int i = 0; i < N; ++i) v[i] = ld128<AUX_SC1>(r, vo, i * STR);
  f32x4 s4;
  for (int s = 0; s < SPIN; ++s) {
    s4 = sumv<N>(v);
    if (__builtin_amdgcn_ballot_w64(__builtin_isunordered(s4[0], s4[1]) | __builtin_isunordered(s4[2], s4[3])) == 0ull) break;
    unsigned m[N], a = 0;
#pragma unroll
    for (int i = 0; i < N; ++i) { m[i] = mx4(v[i]); a = max(a, m[i]); }
    if (__builtin_amdgcn_ballot_w64(a == SENT) == 0ull) break;
    asm volatile("" ::: "memory");
#pragma unroll
    for (int i = 0; i < N; ++i) if (m[i] == SENT) v[i] = ld128<AUX_SC1>(r, vo, i * STR);
  }
  return s4;
}
template <int N, int STR, int SLP> __device__ __forceinline__ void gatherg(v4u (&v)[N], rsrc_t r, int vo, int vlast, unsigned tag) {
#pragma unroll
  for (int i = 0; i < N; ++i) v[i] = i < N - 1 ? ld128<AUX_SC1>(r, vo, i * STR) : ld128<AUX_SC1>(r, vlast, 0);
  for (int s = 0; s < SPIN; ++s) {
    unsigned m[N], a = 0;
#pragma unroll
    for (int i = 0; i < N; ++i) { m[i] = (v[i].y ^ tag) | (v[i].w ^ tag); a |= m[i]; }
    if (__builtin_amdgcn_ballot_w64(a != 0u) == 0ull) break;
    asm volatile("" ::: "memory");
    __builtin_amdgcn_s_sleep(SLP);
#pragma unroll
    for (int i = 0; i < N; ++i) if (m[i] != 0u) v[i] = i < N - 1 ? ld128<AUX_SC1>(r, vo, i * STR) : ld128<AUX_SC1>(r, vlast, 0);
  }
}
__device__ __forceinline__ void until(unsigned t) { while ((int)((unsigned)__builtin_amdgcn_s_memrealtime() - t) < 0) __builtin_amdgcn_s_sleep(1); }
constexpr float LOG2E = 1.4426950408889634f;
constexpr int KR = 64 - NL, PRE = RING + NL;
constexpr int slot(int i) { return (i < KR ? i : i - NL) % RING; }
constexpr int issued(int pr) {
  return pr < 0 ? PRE : PRE + (pr < 2 ? 2 * (pr + 1) : 4 + (pr - 1) / 2) + (pr == 31 ? 2 : 0);
}
constexpr int issuedv(int u) {
  if (u < 0) return issued(31);
  const int a = issued(31) + (u + 1) / 2, b = 64 + u + 1 + TV, m = a > b ? a : b;
  return m < 128 ? m : 128;
}
constexpr int last_issue() { int u = 0; while (issuedv(u) < 128) ++u; return u; }
static_assert(issued(31) - 64 <= RING, "ring");
static_assert(PRE >= 64 || NL == 0, "the parked items are issued before q is needed");

#define STAMP(i)
#define STAMP_OUT()

__global__ void __launch_bounds__(NTHR, 2) fused(const u16* __restrict__ x, const u16* __restrict__ kc, const u16* __restrict__ vc, const u16* __restrict__ wq,
                                                 const u16* __restrict__ wo, const u16* __restrict__ w1, const u16* __restrict__ b1, const u16* __restrict__ w2,
                                                 const u16* __restrict__ b2, const u16* __restrict__ cc, const u16* __restrict__ wm, u16* __restrict__ out,
                                                 unsigned char* ws) {
  extern __shared__ __attribute__((aligned(16))) unsigned char smem[];
  LAS unsigned char* L = (LAS unsigned char*)smem;
  const int wg = blockIdx.x, tid = threadIdx.x, lane = tid & 63, w = __builtin_amdgcn_readfirstlane(tid >> 6);
  const int xl = wg & 7, il = wg >> 3;
  const int kvh = 2 * xl + (il >> 4), bb = (il >> 3) & 1, sp = il & 7, pair = bb * HKV + kvh;
  const rsrc_t rw = mkrs(ws, WS_END);
  const int n16 = lane & 15, g4 = lane >> 4;
  const bool is_wo = il < 9;
  const bool is_q = il >= 9 && il < 17;
  const int qs = 8 * xl + il - 9;
  const int ms = (il - 17) * 8 + xl;
  const bool is_mod = il >= 17 && il < 24 && ms < 54, is_s = is_mod && ms < 18, is_sl = is_q || is_mod;
  const bool is_mlp = il >= 24 || (il >= 13 && il < 23);
  const int mk_ = il >= 24 ? il - 24 : il - 5;
  const int mr = 2 * (9 * xl + (((3 - xl) & 7) + 8 + (mk_ >> 1)) % 9) + (mk_ & 1);
  const bool early1 = mk_ < 2 * KE;
  STAMP(0);
  const unsigned tstart = (unsigned)__builtin_amdgcn_s_memrealtime();
  const unsigned tag = *(const __attribute__((address_space(4))) unsigned*)(uintptr_t)(ws + WS_EPOCH) + 1u;
  if (w >= 4) __builtin_amdgcn_s_setprio(3);

  if (w < 4) {
    const rsrc_t rk = mkrs(kc + ((size_t)pair * SK + sp * 1024) * DH, 262144), rv = mkrs(vc + ((size_t)pair * SK + sp * 1024) * DH, 262144);
    const int vk = w * 1024 + (n16 >> 2) * 256 + g4 * 64 + (n16 & 3) * 16, vv = w * 1024 + lane * 16;
    v4u ring[RING];
    int so = 0;
#define ITEM(i) ({ if ((i) == 64) so = 0; asm volatile("" : "+s"(so)); const v4u r_ = (i) < 64 ? ld128<AUX_NT>(rk, vk, so) : ld128<AUX_NT>(rv, vv, so); so += 4096; r_; })
#define PACE(i) do { if ((i) % PSTEP == 0) { const unsigned due_ = tz + (T1 + (i) * TAU) / 10; while ((int)((unsigned)__builtin_amdgcn_s_memrealtime() - due_) < 0) {} } } while (0)
    unsigned tz = tstart;
    BAR();
    const uint64_t kb = (uint64_t)(kc + ((size_t)pair * SK + sp * 1024) * DH);
    const v4u rk4 = {(unsigned)kb, (unsigned)(kb >> 32) & 0xffffu, 262144u, 0x00020000u};
    const unsigned lk = (unsigned)(size_t)(L + L_PK) + w * (NL * 1024);
#pragma unroll
    for (int i = 0; i < PRE; ++i) {
      PACE(i);
      if (i >= TH) asm volatile("s_waitcnt vmcnt(%0)" :: "n"(TH - 1));
      if (i >= KR && i < 64) {
        unsigned keep;
        asm volatile("s_mov_b32 %0, m0\n\ts_mov_b32 m0, %4\n\ts_nop 0\n\tbuffer_load_dwordx4 %1, %2, %3 offen nt lds\n\ts_mov_b32 m0, %0"
                     : "=&s"(keep) : "v"(vk), "s"(rk4), "s"(so), "s"(lk + (i - KR) * 1024) : "memory");
        so += 4096;
      } else ring[slot(i)] = ITEM(i);
      SB();
    }
    STAMP(1);
    wait_flag(L, F_Q);
    STAMP(2);
    { const unsigned tg = __builtin_amdgcn_readfirstlane(*(LAS unsigned*)(L + L_FLG + 4 * F_T0Q));
      if ((int)(tg - tz) < 0) tz = tg; }
    const v4u qf = *(LAS v4u*)(L + L_Q + (n16 >> 3) * 256 + (4 * g4 + (n16 & 3)) * 16);
    const v4u z4 = {0u, 0u, 0u, 0u};
    const v4u q0 = (lane & 4) ? z4 : qf, q1 = (lane & 4) ? qf : z4;
    float sc[8];
#pragma unroll
    for (int pr = 0; pr < 32; ++pr) {
      f32x4 d = {0.f, 0.f, 0.f, 0.f};
      if (2 * pr < KR) {
        d = mfma(ring[slot(2 * pr)], q0, d);
        d = mfma(ring[slot(2 * pr + 1)], q1, d);
      } else {
        if (2 * pr == KR) VMN(issued(KR / 2 - 1) - 64 < 63 ? issued(KR / 2 - 1) - 64 : 63);
        LAS unsigned char* pk_ = L + L_PK + w * (NL * 1024) + (2 * pr - KR) * 1024 + lane * 16;
        d = mfma(*(LAS v4u*)pk_, q0, d);
        d = mfma(*(LAS v4u*)(pk_ + 1024), q1, d);
      }
      SB();
#pragma unroll
      for (int i = issued(pr - 1); i < issued(pr); ++i) {
        PACE(i);
        asm volatile("s_waitcnt vmcnt(%0)" :: "n"(TH - 1));
        ring[slot(i)] = ITEM(i);
      }
      const float s = (dppf<0x00>(d[0]) + dppf<0x55>(d[1])) + (dppf<0xAA>(d[2]) + dppf<0xFF>(d[3]));
      sc[pr >> 2] = (pr & 3) == 0 ? s : ((lane & 3) == (pr & 3) ? s : sc[pr >> 2]);
      asm volatile("" : "+v"(sc[pr >> 2]));
      SB();
    }
    STAMP(3);
    float m = -INFINITY;
#pragma unroll
    for (int a = 0; a < 8; ++a) { sc[a] = rb(rb(sc[a]) * 0.08838834764831845f); m = fmaxf(m, sc[a]); }
    m = x32_max(x16_max(max8(m)));
    float l = 0.f;
    unsigned pp[8], pm[8];
#pragma unroll
    for (int a = 0; a < 8; ++a) { const float p = __builtin_amdgcn_exp2f((sc[a] - m) * LOG2E); l += p; pp[a] = pk(p, p); pm[a] = dppu<HMIR>(pp[a]); }
    l = x32_sum(x16_sum(sum8(l)));
    if ((lane & 55) == 0) *(LAS f32x2*)(L + L_WML + w * 16 + (lane >> 3) * 8) = f32x2{m, l};
    set_flag(L, F_ML0 + w);
    const unsigned hm = (lane & 1) ? 0xffff0000u : 0x0000ffffu;
    const int ci = (lane & 7) >> 1;
    const unsigned mk0 = ci == 0 ? hm : 0u, mk1 = ci == 1 ? hm : 0u, mk2 = ci == 2 ? hm : 0u, mk3 = ci == 3 ? hm : 0u;
    f32x4 acc = {0.f, 0.f, 0.f, 0.f};
    SB();
#pragma unroll
    for (int u = 0; u < 64; ++u) {
      const int a = u >> 3, v0 = (u >> 1) & 3, t0 = u & 1;
      unsigned own, oth;
      switch (v0) {
        case 0: own = dppu<0x00>(pp[a]); oth = dppu<0xFF>(pm[a]); break;
        case 1: own = dppu<0x55>(pp[a]); oth = dppu<0xAA>(pm[a]); break;
        case 2: own = dppu<0xAA>(pp[a]); oth = dppu<0x55>(pm[a]); break;
        default: own = dppu<0xFF>(pp[a]); oth = dppu<0x00>(pm[a]); break;
      }
      const unsigned lo = t0 == 0 ? own : oth, hi = t0 == 0 ? oth : own;
      const v4u bp = {lo & mk0, lo & mk1, hi & mk2, hi & mk3};
      acc = mfma(ring[slot(64 + u)], bp, acc);
      SB();
#pragma unroll
      for (int i = issuedv(u - 1); i < issuedv(u); ++i) {
        PACE(i);
        asm volatile("s_waitcnt vmcnt(%0)" :: "n"(TH - 1));
        ring[slot(i)] = ITEM(i);
      }
      if (u == TRIG) *(volatile LAS unsigned*)(L + L_FLG + 4 * (F_V0 + w)) = 1u;
      SB();
    }
    *(LAS v4u*)(L + L_MRG + w * 1024 + lane * 16) = nos4(acc);
    STAMP(4);
  } else {
    const int hw = w - 4;
    float xv[12];
    const int ab = hw >> 1, lq = lane < 48 ? lane : 47;
    if (!(w & 1)) {
      const v4u s4 = {SENT, SENT, SENT, SENT};
      if (w == 6) {
#pragma unroll
        for (int i = 0; i < 4; ++i) *(LAS v4u*)(L + L_MRG + i * 1024 + lane * 16) = s4;
        if (lane < 32) *(LAS unsigned*)(L + L_FLG + lane * 4) = 0u;
        if (lane < 4) *(LAS unsigned*)(L + L_H1 + 128 + lane * 4) = 0u;
      }
      if (w == 6) {
        if (is_s) {
#pragma unroll
          for (int i = 0; i < 3; ++i) st128(rw, i < 2 || lane < 32 ? WS_MOD + ms * 2560 + i * 1024 + lane * 16 : WS_END, s4);
        }
        if (is_q) {
          const int pq = ((qs >> 1) & 1) * HKV + (qs >> 2);
#pragma unroll
          for (int i = 0; i < 4; ++i) st128(rw, WS_H1 + (pq * 8 + (qs & 1) * 4 + i) * 1024 + lane * 16, s4);
          st128(rw, lane < 4 ? WS_ML + (pq * 8 + (qs & 1) * 4 + lane) * 16 : WS_END, s4);
        }
        if (is_mlp) {
#pragma unroll
          for (int i = 0; i < 5; ++i) st128(rw, i < 4 || lane < 32 ? WS_H3 + mr * 4608 + i * 1024 + lane * 16 : WS_END, s4);
        }
      }
      if (!is_s) BAR();
      if (is_sl || is_wo) {
        const rsrc_t rm = is_q ? mkrs(wq, D * NQ * 2) : is_wo ? mkrs(wo + (size_t)xl * 512 * D, 512 * D * 2) : mkrs(wm, D * 6 * D * 2);
        const int rs_ = is_q ? 8192 : is_wo ? 1152 : 6912, n_ = is_wo ? 32 : 36;
        const int sw = is_wo ? 0 : (((lane >> 4) & 1) | (ab << 1)) << 1;
        const int vm = (ab * 8 + (lane >> 3)) * rs_ + (is_q ? qs : is_wo ? il : ms) * 128 + ((lane & 7) ^ sw) * 16;
        if (TWE && !is_s) until(tstart + TWE / 10);
#pragma clang loop unroll(disable)
        for (int i = 0; i < n_; ++i) dma(rm, L + L_WT + ab * 1024 + i * 2048, vm, i * 16 * rs_);
      } else if (is_mlp && early1) {
        const rsrc_t r1 = mkrs(w1, D * F * 2);
        const int v1 = (ab * 16 + (lane >> 2)) * 9216 + mr * 64 + (lane & 3) * 16;
        if (TWE) until(tstart + TWE / 10);
#pragma clang loop unroll(disable)
        for (int i = 0; i < 18; ++i) dma(r1, L + L_WT + ab * 1024 + i * 2048, v1, i * 32 * 9216);
      }
      if (is_s) BAR();
      VM0();
      set_flag(L, F_D0 + ab);
      STAMP(1);
    } else {
      const rsrc_t rx = mkrs(x, 2304), rc = mkrs(cc, 2304);
      v2u xr[3], cr[3];
#pragma unroll
      for (int i = 0; i < 3; ++i) cr[i] = ld64<0>(rc, is_mod ? ab * 1152 + lq * 8 : 2304, i * 384);
#pragma unroll
      for (int i = 0; i < 3; ++i) xr[i] = ld64<0>(rx, is_q || is_mlp ? ab * 1152 + lq * 8 : 2304, i * 384);
      BAR();
      if (is_mod) {
#pragma unroll
        for (int i = 0; i < 3; ++i) {
          float a[4] = {bf_lo(cr[i].x), bf_hi(cr[i].x), bf_lo(cr[i].y), bf_hi(cr[i].y)};
#pragma unroll
          for (int j = 0; j < 4; ++j) a[j] *= __builtin_amdgcn_rcpf(1.f + __builtin_amdgcn_exp2f(-a[j] * LOG2E));
          *(LAS v2u*)(L + L_ACT + ab * 1152 + i * 384 + lq * 8) = v2u{pk(a[0], a[1]), pk(a[2], a[3])};
        }
      }
#pragma unroll
      for (int i = 0; i < 3; ++i) *(LAS v2u*)(L + L_X + ab * 1152 + i * 384 + lq * 8) = xr[i];
      if (!is_q) set_flag(L, F_ACT0 + ab);
#pragma unroll
      for (int i = 0; i < 3; ++i) { xv[4 * i] = bf_lo(xr[i].x); xv[4 * i + 1] = bf_hi(xr[i].x); xv[4 * i + 2] = bf_lo(xr[i].y); xv[4 * i + 3] = bf_hi(xr[i].y); }
      if (is_q) {
        float s = 0.f;
#pragma unroll
        for (int i = 0; i < 12; ++i) s += xv[i];
        const float mean = sum64(lane < 48 ? s : 0.f) * (1.f / D);
        float q = 0.f;
#pragma unroll
        for (int i = 0; i < 12; ++i) { const float a = xv[i] - mean; q += a * a; }
        const float var = sum64(lane < 48 ? q : 0.f) * (1.f / D);
        const float den = rb(__builtin_sqrtf(rb(rb(var) + 1e-5f))), mb = rb(mean);
#pragma unroll
        for (int i = 0; i < 12; ++i) { xv[i] = rb(rb(xv[i] - mb) / den); PIN(xv[i]); }
      }
    }
    v2u modv = {0u, 0u};
    if (is_sl) {
      wait_flag(L, F_D0);
      wait_flag(L, F_D1);
      v4u a[18];
      LAS unsigned char* pa = L + L_WT + (8 * g4 + (n16 >> 2)) * 128 + ((hw ^ ((n16 >> 3) | ((g4 & 1) << 1))) * 32) + 8 * (lane & 3);
#pragma unroll
      for (int s = 0; s < 18; ++s) a[s] = trd2(pa + 32 * s * 128, pa + (32 * s + 4) * 128);
      if (is_q && (w & 1)) {
        v4u sg[7];
        until(tstart + TSG / 10);
        gatherg<7, 768, 8>(sg, rw, WS_SG + (ab * 288 + lq) * 16, WS_T0, tag);
        STAMP(2);
        *(LAS unsigned*)(L + L_FLG + 4 * F_T0) = sg[6].x;
#pragma unroll
        for (int i = 0; i < 3; ++i) {
          const f32x2 h01 = rb2(f32x2{xv[4 * i], xv[4 * i + 1]} * rb2(unpk(sg[3 + i].x) + 1.f)) + unpk(sg[i].x);
          const f32x2 h23 = rb2(f32x2{xv[4 * i + 2], xv[4 * i + 3]} * rb2(unpk(sg[3 + i].z) + 1.f)) + unpk(sg[i].z);
          *(LAS v2u*)(L + L_ACT + ab * 1152 + i * 384 + lq * 8) = v2u{pk(h01.x, h01.y), pk(h23.x, h23.y)};
        }
        set_flag(L, F_ACT0 + ab);
      }
      wait_flag(L, F_ACT0);
      wait_flag(L, F_ACT1);
      LAS unsigned char* p = L + L_ACT + (lane & 1) * 1152 + 16 * g4;
      v4u bq[18];
#pragma unroll
      for (int s = 0; s < 18; ++s) bq[s] = *(LAS v4u*)(p + 64 * s);
      SB();
      f32x4 d = {0.f, 0.f, 0.f, 0.f};
#pragma unroll
      for (int s = 0; s < 18; ++s) d = mfma(a[s], bq[s], d);
      const unsigned p01 = pk(d[0], d[1]), p23 = pk(d[2], d[3]);
      if (is_q || is_s) {
        const int off = is_q ? WS_QG + (n16 * 1024 + qs * 16 + hw * 4 + g4) * 16 : WS_SG + (n16 * 288 + ms * 16 + hw * 4 + g4) * 16;
        st128(rw, n16 < 2 ? off : WS_END, v4u{p01, tag, p23, tag});
        if (w == 4 && (is_q ? (qs & 3) == 0 : ms == 0)) {
          const unsigned tz = is_q ? *(LAS unsigned*)(L + L_FLG + 4 * F_T0) : tstart;
          st128(rw, lane == 0 ? (is_q ? WS_TQ + (qs >> 2) * 128 : WS_T0) : WS_END, v4u{tz, tag, tz, tag});
        }
        if (w == 4) { if (is_s) { STAMP(2); } else { STAMP(5); } }
      } else modv = v2u{nos(p01), nos(p23)};
    }
    set_flag(L, F_S0 + hw);
    if (w == 6) {
      v4u qg[2];
      until(tstart + TQP / 10);
      gatherg<2, 0, 8>(qg, rw, WS_QG + (bb * 1024 + kvh * 64 + lane) * 16, WS_TQ + kvh * 128, tag);
      *(LAS v2u*)(L + L_Q + lane * 8) = v2u{qg[0].x, qg[0].z};
      *(LAS unsigned*)(L + L_FLG + 4 * F_T0Q) = qg[1].x;
      set_flag(L, F_Q);
      STAMP(2);
    }
    if (is_mod && !is_s) {
      wait_flag(L, F_Q);
      st64(rw, n16 < 2 ? WS_MOD + n16 * 4608 + ((ms - 18) * 64 + hw * 16 + g4 * 4) * 2 : WS_END, modv);
    }
    if (w == 6) {
      if (is_wo) {
        v4u qg[2];
        gatherg<2, 0, 32>(qg, rw, WS_QG + ((kvh ^ 1) * 64 + (lane & 60)) * 16, WS_TQ + kvh * 128, tag);
      }
      if (is_mlp) {
        const unsigned bv = lane < 16 ? ld32<0>(mkrs(b1, F * 2), mr * 64 + lane * 4, 0) : ld32<0>(mkrs(b2, D * 2), (mr % 72) * 16 + (lane & 3) * 4, 0);
        if (lane < 20) *(LAS unsigned*)(L + L_BI + lane * 4) = bv;
        v4u mv[9];
        until(tstart + TMG / 10);
        for (int s = 0; s < SPIN; ++s) {
          asm volatile("" ::: "memory");
          unsigned a = 0;
#pragma unroll
          for (int i = 0; i < 9; ++i) mv[i] = ld128<AUX_SC1>(rw, WS_MOD + lane * 16, i * 1024);
#pragma unroll
          for (int i = 0; i < 9; ++i) a = max(a, mx4(mv[i]));
          if (__builtin_amdgcn_ballot_w64(a == SENT) == 0ull) break;
          __builtin_amdgcn_s_sleep(32);
        }
#pragma unroll
        for (int i = 0; i < 9; ++i) *(LAS v4u*)(L + L_MOD + i * 1024 + lane * 16) = mv[i];
        wait_flag(L, F_ACT0);
        wait_flag(L, F_ACT1);
      }
      wait_flag(L, F_D0);
      set_flag(L, F_RDY);
      STAMP(3);
    }
  }

  if (w == 4) {
    wait_flag4(L, F_ML0);
    const int hd = (lane >> 3) & 1;
    f32x2 ml[4];
#pragma unroll
    for (int i = 0; i < 4; ++i) ml[i] = *(LAS f32x2*)(L + L_WML + i * 16 + hd * 8);
    const float mc = fmaxf(fmaxf(ml[0].x, ml[1].x), fmaxf(ml[2].x, ml[3].x));
    float lc = 0.f;
#pragma unroll
    for (int i = 0; i < 4; ++i) lc += ml[i].y * __builtin_amdgcn_exp2f((ml[i].x - mc) * LOG2E);
    if ((lane & 55) == 0) st64(rw, WS_ML + (pair * 8 + sp) * 16 + hd * 8, v2u{nos(__float_as_uint(mc)), nos(__float_as_uint(lc))});
    v2u r;
    for (int s = 0; s < SPIN; ++s) {
      asm volatile("" ::: "memory");
      __builtin_amdgcn_s_sleep(32);
      r = ld64<AUX_SC1>(rw, WS_ML + (pair * 8 + (lane & 7)) * 16 + hd * 8, 0);
      if (__builtin_amdgcn_ballot_w64(max(r.x, r.y) == SENT) == 0ull) break;
    }
    const float ms_ = __uint_as_float(r.x), M = max8(ms_);
    const float Lt = sum8(__uint_as_float(r.y) * __builtin_amdgcn_exp2f((ms_ - M) * LOG2E));
    const float inv = 1.f / Lt;
    float fw[4];
#pragma unroll
    for (int i = 0; i < 4; ++i) { fw[i] = __builtin_amdgcn_exp2f((ml[i].x - M) * LOG2E) * inv; PIN(fw[i]); }
    int vo = WS_H1 + (pair * 8 + sp) * 1024 + lane * 16;
    PIN(vo);
    STAMP(3);
    wait_flag4(L, F_V0);
    v4u pv[4];
    for (;;) {
#pragma unroll
      for (int i = 0; i < 4; ++i) pv[i] = *(volatile LAS v4u*)(L + L_MRG + i * 1024 + lane * 16);
      if (__builtin_amdgcn_ballot_w64(max(max(mx4(pv[0]), mx4(pv[1])), max(mx4(pv[2]), mx4(pv[3]))) == SENT) == 0ull) break;
    }
    st128(rw, vo, nos4((asf(pv[0]) * fw[0] + asf(pv[1]) * fw[1]) + (asf(pv[2]) * fw[2] + asf(pv[3]) * fw[3])));
    STAMP(6);
  } else if ((w & 1) && w > 4 && is_mlp) {
    const int lw = (w - 5) >> 1;
    const rsrc_t r1 = mkrs(w1, D * F * 2), r2 = mkrs(w2, D * F * 2);
    const int v1 = (lw * 16 + (lane >> 2)) * 9216 + mr * 64 + (lane & 3) * 16;
    const int v2 = mr * 36864 + lw * 1024 + lane * 16;
    wait_flag4(L, F_S0);
    wait_flag4(L, F_V0);
    if (TWM) until((unsigned)__builtin_amdgcn_s_memrealtime() + TWM / 10);
    STAMP(4);
    if (!early1) {
#pragma clang loop unroll(disable)
      for (int i = 0; i < 18; ++i) dma(r1, L + L_WT + lw * 1024 + i * 2048, v1, i * 32 * 9216);
    }
#pragma clang loop unroll(disable)
    for (int i = 0; i < 18; ++i) dma(r2, L + L_W2 + lw * 1024 + i * 2048, v2, i * 2048);
    VM0();
    set_flag(L, F_W6 + lw);
    STAMP(5);
  }
  if (is_wo) {
    wait_flag(L, F_RDY);
    if (w < 4) {
      __builtin_amdgcn_s_sleep(S1);
      const f32x4 o = gather<8, 1024>(rw, WS_H1 + ((w >> 1) * HKV + 2 * xl + (w & 1)) * 8192 + lane * 16);
      *(LAS v2u*)(L + L_O + w * 512 + lane * 8) = v2u{pk(o[0], o[1]), pk(o[2], o[3])};
      BAR();
      STAMP(7);
    } else {
      v4u a[16];
      LAS unsigned char* p = L + L_WT + (w - 4) * 32 + (8 * (n16 >> 2) + 2 * g4) * 128 + 8 * (lane & 3);
#pragma unroll
      for (int s = 0; s < 16; ++s) { const int r0 = (s >> 3) * 256 + (s & 1) * 128 + ((s >> 1) & 3) * 32; a[s] = trd2(p + r0 * 128, p + (r0 + 1) * 128); }
      unsigned pb = (unsigned)(size_t)(L + L_O) + (lane & 1) * 1024 + g4 * 16;
      int vo = n16 < 2 ? WS_H2 + xl * 4608 + n16 * 2304 + (il * 64 + (w - 4) * 16 + g4 * 4) * 4 : WS_END;
      PIN(pb); PIN(vo);
      STAMP(7);
      BAR();
      v4u bq[16];
#pragma unroll
      for (int s = 0; s < 16; ++s) bq[s] = *(LAS v4u*)(size_t)(pb + 64 * s);
      SB();
      f32x4 acc = {0.f, 0.f, 0.f, 0.f};
#pragma unroll
      for (int s = 0; s < 16; ++s) acc = mfma(a[s], bq[s], acc);
      st128(rw, vo, nos4(acc));
      STAMP(8);
    }
  } else if (is_mlp) {
    wait_flag(L, F_RDY);
    wait_flag(L, F_W6);
    wait_flag(L, F_W7);
    const int t0_ = w < 4 ? 5 * w : 4 + 4 * w, nt_ = w < 4 ? 5 : 4;
    unsigned ph[5];
#pragma unroll
    for (int t = 0; t < 5; ++t) { ph[t] = (unsigned)(size_t)(L + L_H1) + ((n16 >> 1) == t ? (lane & 1) * 64 + g4 * 16 : 128); PIN(ph[t]); }
    int v3 = (n16 >> 1) < nt_ ? WS_H3 + mr * 4608 + (lane & 1) * 2304 + ((t0_ + (n16 >> 1)) * 16 + g4 * 4) * 4 : WS_END;
    PIN(v3);
    LAS unsigned char* p2 = L + L_W2 + (8 * g4 + (n16 >> 2)) * 1152 + t0_ * 32 + 8 * (lane & 3);
#define X1_PART() do { \
      const int t_ = w * 64 + lane, t = t_ < 288 ? t_ : 287, tb = t >= 144 ? 1 : 0; \
      const v2u xq = *(LAS v2u*)(L + L_X + t * 8), gq = *(LAS v2u*)(L + L_MOD + tb * 4608 + (t - 144 * tb) * 8); \
      f32x2 x01 = unpk(xq.x), x23 = unpk(xq.y), g01 = unpk(gq.x), g23 = unpk(gq.y); \
      unsigned px = (unsigned)(size_t)(L + L_X1) + t * 16; \
      PIN(x01); PIN(x23); PIN(g01); PIN(g23); PIN(px); \
      const f32x4 s = gather<8, 4608>(rw, WS_H2 + t * 16); \
      const f32x2 r01 = rb2(x01 + rb2(g01 * rb2(f32x2{s[0], s[1]}))), r23 = rb2(x23 + rb2(g23 * rb2(f32x2{s[2], s[3]}))); \
      *(LAS f32x4*)(size_t)px = f32x4{r01.x, r01.y, r23.x, r23.y}; \
    } while (0)
#define W2_PART(NT) do { \
      v4u bq[NT]; \
      _Pragma("unroll") for (int t = 0; t < NT; ++t) bq[t] = *(LAS v4u*)(size_t)ph[t]; \
      SB(); \
      f32x4 acc = {0.f, 0.f, 0.f, 0.f}; \
      _Pragma("unroll") for (int t = 0; t < NT; ++t) acc = mfma(a2[t], bq[t], acc); \
      st128(rw, v3, nos4(acc)); \
    } while (0)
    if (w < 4) {
      const int b_ = w >> 1, hf = w & 1;
      float c1[5], sbv[5];
#pragma unroll
      for (int i = 0; i < 5; ++i) {
        const int k = min(lane + 64 * (5 * hf + i), 575);
        c1[i] = rb(1.f + bf_lo(*(LAS u16*)(L + L_MOD + b_ * 4608 + (1152 + k) * 2)));
        sbv[i] = bf_lo(*(LAS u16*)(L + L_MOD + b_ * 4608 + (576 + k) * 2));
        PIN(c1[i]); PIN(sbv[i]);
      }
      unsigned pr_ = (unsigned)(size_t)(L + L_X1) + b_ * 2304 + lane * 4, pw_ = (unsigned)(size_t)(L + L_H2) + b_ * 1280 + (lane + 320 * hf) * 2;
      PIN(pr_); PIN(pw_);
      X1_PART();
      STAMP(9);
      BAR();
      float xv[9];
#pragma unroll
      for (int i = 0; i < 9; ++i) xv[i] = *(LAS float*)(size_t)(pr_ + 256 * i);
      SB();
      const float pv_ = __builtin_bit_cast(float, __builtin_amdgcn_readfirstlane(__builtin_bit_cast(int, xv[0])));
      float dv[9];
#pragma unroll
      for (int i = 0; i < 9; ++i) dv[i] = xv[i] - pv_;
      float s1 = ((dv[0] + dv[1]) + (dv[2] + dv[3])) + ((dv[4] + dv[5]) + (dv[6] + dv[7])) + dv[8];
      float s2 = (dv[0] * dv[0] + dv[1] * dv[1] + dv[2] * dv[2]) + (dv[3] * dv[3] + dv[4] * dv[4] + dv[5] * dv[5]) + (dv[6] * dv[6] + dv[7] * dv[7] + dv[8] * dv[8]);
      s1 = sum64(s1) * (1.f / D); s2 = sum64(s2) * (1.f / D);
      const float var = fmaxf(s2 - s1 * s1, 0.f), mb = rb(pv_ + s1);
      const float rden = __builtin_amdgcn_rcpf(rb(__builtin_amdgcn_sqrtf(rb(rb(var) + 1e-5f))));
      float own[6];
#pragma unroll
      for (int i = 0; i < 5; ++i) own[i] = (hf && i < 4) ? xv[5 + i] : xv[i];
      own[5] = own[4];
#pragma unroll
      for (int i = 0; i < 3; ++i) {
        const f32x2 cc2 = {c1[2 * i], c1[i < 2 ? 2 * i + 1 : 4]}, ss2 = {sbv[2 * i], sbv[i < 2 ? 2 * i + 1 : 4]};
        const f32x2 h = rb2(rb2(rb2(f32x2{own[2 * i], own[2 * i + 1]} - mb) * rden) * cc2) + ss2;
        const unsigned hp_ = pk(h.x, h.y);
        *(LAS u16*)(size_t)(pw_ + 256 * i) = (u16)hp_;
        if (i < 2) *(LAS u16*)(size_t)(pw_ + 256 * i + 128) = (u16)(hp_ >> 16);
      }
      BAR();
      v4u a2[5];
#pragma unroll
      for (int t = 0; t < 5; ++t) a2[t] = trd2(p2 + 32 * t, p2 + 32 * t + 4 * 1152);
      BAR();
      W2_PART(5);
      STAMP(10);
    } else {
      v4u a1[9], a2[4];
      LAS unsigned char* p1 = L + L_WT + (((lane >> 1) & 1) * 288 + 8 * g4 + (n16 >> 2)) * 64 + (w - 4) * 16 + 8 * (lane & 1);
#pragma unroll
      for (int s = 0; s < 9; ++s) a1[s] = trd2(p1 + 32 * s * 64, p1 + (32 * s + 4) * 64);
#pragma unroll
      for (int t = 0; t < 4; ++t) a2[t] = trd2(p2 + 32 * t, p2 + 32 * t + 4 * 1152);
      const v2u tb1 = *(LAS v2u*)(L + L_BI + ((w - 4) * 8 + (g4 & 1) * 4) * 2);
      f32x2 b01 = unpk(tb1.x), b23 = unpk(tb1.y);
      unsigned pb = (unsigned)(size_t)(L + L_H2) + (lane & 1) * 1280 + ((lane >> 1) & 1) * 576 + 16 * g4;
      unsigned pw_ = (unsigned)(size_t)(L + L_H1) + (lane & 1) * 64 + ((w - 4) * 8 + (g4 & 1) * 4) * 2;
      PIN(b01); PIN(b23); PIN(pb); PIN(pw_);
      STAMP(7);
      if (w == 4) X1_PART();
      BAR();
      BAR();
      {
        v4u bq[9];
#pragma unroll
        for (int s = 0; s < 9; ++s) bq[s] = *(LAS v4u*)(size_t)(pb + 64 * s);
        SB();
        f32x4 d = {0.f, 0.f, 0.f, 0.f};
#pragma unroll
        for (int s = 0; s < 9; ++s) d = mfma(a1[s], bq[s], d);
        float e[4];
#pragma unroll
        for (int i = 0; i < 4; ++i) {
          const unsigned sh = dppu<QX2>(__float_as_uint(d[i]));
          e[i] = d[i] + __uint_as_float(__builtin_amdgcn_permlane32_swap(sh, sh, false, false)[1]);
        }
        f32x2 r01 = rb2(rb2(f32x2{e[0], e[1]}) + b01), r23 = rb2(rb2(f32x2{e[2], e[3]}) + b23);
        r01.x = r01.x > 0.f ? r01.x : 0.f; r01.y = r01.y > 0.f ? r01.y : 0.f; r23.x = r23.x > 0.f ? r23.x : 0.f; r23.y = r23.y > 0.f ? r23.y : 0.f;
        r01 *= r01; r23 *= r23;
        if ((lane & 46) == 0) *(LAS v2u*)(size_t)pw_ = v2u{pk(r01.x, r01.y), pk(r23.x, r23.y)};
      }
      BAR();
      W2_PART(4);
      if (w == 7) {
        const int ob = mr >= 72 ? 1 : 0, oc = (mr - 72 * ob) * 8 + (lane & 1) * 4;
        const v2u tb2 = *(LAS v2u*)(L + L_BI + 64 + (lane & 1) * 8), tgb = *(LAS v2u*)(L + L_MOD + ob * 4608 + (1728 + oc) * 2);
        const f32x4 xo = *(LAS f32x4*)(L + L_X1 + mr * 32 + (lane & 1) * 16);
        f32x2 c01 = unpk(tb2.x), c23 = unpk(tb2.y), g01 = unpk(tgb.x), g23 = unpk(tgb.y), x01 = {xo[0], xo[1]}, x23 = {xo[2], xo[3]};
        int vout = lane < 2 ? mr * 16 + lane * 8 : 2304;
        PIN(c01); PIN(c23); PIN(g01); PIN(g23); PIN(x01); PIN(x23); PIN(vout);
        __builtin_amdgcn_s_sleep(H3SLEEP);
        f32x4 t = gather<5, 32 * 4608>(rw, WS_H3 + (lane >> 1) * 4608 + mr * 32 + (lane & 1) * 16);
        STAMP(10);
#pragma unroll
        for (int i = 0; i < 4; ++i) t[i] += dppf<QX2>(t[i]);
#pragma unroll
        for (int i = 0; i < 4; ++i) t[i] += dppf<ROR4>(t[i]);
#pragma unroll
        for (int i = 0; i < 4; ++i) t[i] += dppf<ROR8>(t[i]);
#pragma unroll
        for (int i = 0; i < 4; ++i) t[i] = x16_sum(t[i]);
#pragma unroll
        for (int i = 0; i < 4; ++i) t[i] = x32_sum(t[i]);
        const f32x2 r01 = x01 + rb2(g01 * rb2(rb2(f32x2{t[0], t[1]}) + c01)), r23 = x23 + rb2(g23 * rb2(rb2(f32x2{t[2], t[3]}) + c23));
        __builtin_amdgcn_raw_buffer_store_b64(v2u{pk(r01.x, r01.y), pk(r23.x, r23.y)}, mkrs(out, 2304), vout, 0, 0);
        STAMP(11);
      } else if (w == 6 && mr == 0 && lane == 0) {
        __hip_atomic_store((GAS unsigned*)ws, tag, __ATOMIC_RELAXED, __HIP_MEMORY_SCOPE_AGENT);
      }
    }
  }
  STAMP_OUT();
}
}

extern "C" void kernel_launch(void* const* d_in, const int* in_sizes, int n_in, void* d_out, int out_size, void* d_ws, size_t ws_size, hipStream_t stream) {
  using mk::u16;
  (void)hipFuncSetAttribute((const void*)mk::fused, hipFuncAttributeMaxDynamicSharedMemorySize, mk::L_END);
  mk::fused<<<256, mk::NTHR, mk::L_END, stream>>>((const u16*)d_in[0], (const u16*)d_in[1], (const u16*)d_in[2], (const u16*)d_in[3], (const u16*)d_in[4],
                                                  (const u16*)d_in[6], (const u16*)d_in[7], (const u16*)d_in[8], (const u16*)d_in[9], (const u16*)d_in[10],
                                                  (const u16*)d_in[11], (u16*)d_out, (unsigned char*)d_ws + WSOFF);
}
```

```cpp
#define TSG 5500
#define TQP 8500
#define TH 10
#define TV 10
#define T1 2800
#define TAU 140
#define PROBE_J 0
#define PROBE_W 1
#define NL 0
#include <hip/hip_runtime.h>
#include <hip/hip_bf16.h>
#include <stdint.h>

namespace mk {
typedef unsigned short u16;
typedef unsigned v4u __attribute__((ext_vector_type(4)));
typedef unsigned v2u __attribute__((ext_vector_type(2)));
typedef float f32x4 __attribute__((ext_vector_type(4)));
typedef float f32x2 __attribute__((ext_vector_type(2)));
typedef short s16x4 __attribute__((ext_vector_type(4)));
typedef __bf16 bf16x2 __attribute__((ext_vector_type(2)));
typedef __bf16 bf16x8 __attribute__((ext_vector_type(8)));
typedef __amdgpu_buffer_rsrc_t rsrc_t;
#define LAS __attribute__((address_space(3)))
#define GAS __attribute__((address_space(1)))

constexpr int D = 576, F = 4608, SK = 8192, DH = 128, HKV = 16, NQ = 4096;
constexpr int NTHR = 512;
constexpr int AUX_NT = 2, AUX_SC1 = 16;
#define RING 56
#define PSTEP 2
#define TRIGW last_issue()
#define TMS 0
#define TQS 0
#define TMG 14000
#define CLRPOS 0
#define WSOFF 0
#define S1 8
#define TRIG last_issue()
#define LTH 32
#define LTHW 32
#define H3SLEEP 12
#define KE 4
#define TWE 0
#define TWM 0
constexpr unsigned SENT = 0xffffffffu;
constexpr int SPIN = 1 << 16;

constexpr int WS_EPOCH = 0;
constexpr int WS_SG = 256;
constexpr int WS_T0 = WS_SG + 9216;
constexpr int WS_TQ = WS_T0 + 128;
constexpr int WS_QG = 16384;
constexpr int WS_MOD = WS_QG + 32768;
constexpr int WS_H2 = WS_MOD + 9216;
constexpr int WS_ML = WS_H2 + 36864;
constexpr int WS_H1 = WS_ML + 4096;
constexpr int WS_H3 = WS_H1 + 262144;
constexpr int WS_END = WS_H3 + 144 * 4608;
constexpr int WS_DBG = 4 << 20;

constexpr int L_WT = 0;
constexpr int L_W2 = L_WT + 36864;
constexpr int L_PK = L_WT + 73728;
constexpr int L_ACT = L_PK + NL * 4096;
constexpr int L_Q = L_ACT + 2304;
constexpr int L_MRG = L_Q + 512;
constexpr int L_WML = L_MRG + 4096;
constexpr int L_FLG = L_WML + 64;
constexpr int L_MOD = L_FLG + 128;
constexpr int L_X = L_MOD + 9216;
constexpr int L_X1 = L_X + 2304;
constexpr int L_H2 = L_X1 + 4608;
constexpr int L_O = L_H2 + 2560;
constexpr int L_H1 = L_O + 2048;
constexpr int L_BI = L_H1 + 128 + 16;
constexpr int L_END = L_BI + 80;
static_assert(L_END <= 160 * 1024, "LDS");
enum { F_Q = 0, F_RDY = 1, F_ACT0 = 2, F_ACT1 = 3, F_ML0 = 4  , F_V0 = 8  , F_W6 = 12, F_W7 = 13, F_D0 = 14, F_D1 = 15, F_T0 = 16, F_T0Q = 17, F_S0 = 20   };

__device__ __forceinline__ float bf_lo(unsigned u) { return __uint_as_float(u << 16); }
__device__ __forceinline__ float bf_hi(unsigned u) { return __uint_as_float(u & 0xffff0000u); }
__device__ __forceinline__ unsigned pk(float a, float b) { f32x2 v = {a, b}; return __builtin_bit_cast(unsigned, __builtin_convertvector(v, bf16x2)); }
__device__ __forceinline__ float rb(float a) { return bf_lo(pk(a, a)); }
__device__ __forceinline__ unsigned nos(unsigned u) { return u < 0xfffffffeu ? u : 0xfffffffeu; }
__device__ __forceinline__ v4u nos4(f32x4 v) { return v4u{nos(__float_as_uint(v[0])), nos(__float_as_uint(v[1])), nos(__float_as_uint(v[2])), nos(__float_as_uint(v[3]))}; }
__device__ __forceinline__ f32x4 asf(v4u v) { return __builtin_bit_cast(f32x4, v); }
__device__ __forceinline__ rsrc_t mkrs(const void* p, unsigned bytes) { return __builtin_amdgcn_make_buffer_rsrc((void*)p, 0, (int)bytes, 0x00020000); }
template <int AUX> __device__ __forceinline__ v4u ld128(rsrc_t r, int v, int s) { return __builtin_amdgcn_raw_buffer_load_b128(r, v, s, AUX); }
template <int AUX> __device__ __forceinline__ v2u ld64(rsrc_t r, int v, int s) { return __builtin_amdgcn_raw_buffer_load_b64(r, v, s, AUX); }
template <int AUX> __device__ __forceinline__ unsigned ld32(rsrc_t r, int v, int s) { return __builtin_amdgcn_raw_buffer_load_b32(r, v, s, AUX); }
template <int AUX> __device__ __forceinline__ unsigned ld16(rsrc_t r, int v, int s) { return __builtin_amdgcn_raw_buffer_load_b16(r, v, s, AUX); }
__device__ __forceinline__ void st128(rsrc_t r, int v, v4u d) { __builtin_amdgcn_raw_buffer_store_b128(d, r, v, 0, AUX_SC1); }
__device__ __forceinline__ void st64(rsrc_t r, int v, v2u d) { __builtin_amdgcn_raw_buffer_store_b64(d, r, v, 0, AUX_SC1); }
__device__ __forceinline__ void dma(rsrc_t r, LAS unsigned char* l, int v, int s) { __builtin_amdgcn_raw_ptr_buffer_load_lds(r, (LAS void*)l, 16, v, s, 0, AUX_NT); }
__device__ __forceinline__ f32x4 mfma(v4u a, v4u b, f32x4 c) { return __builtin_amdgcn_mfma_f32_16x16x32_bf16(__builtin_bit_cast(bf16x8, a), __builtin_bit_cast(bf16x8, b), c, 0, 0, 0); }
__device__ __forceinline__ v2u trd(LAS unsigned char* p) { return __builtin_bit_cast(v2u, __builtin_amdgcn_ds_read_tr16_b64_v4i16((LAS s16x4*)p)); }
__device__ __forceinline__ v4u trd2(LAS unsigned char* p0, LAS unsigned char* p1) { v2u a = trd(p0), b = trd(p1); return v4u{a.x, a.y, b.x, b.y}; }
template <int C> __device__ __forceinline__ float dppf(float x) { return __builtin_bit_cast(float, __builtin_amdgcn_mov_dpp(__builtin_bit_cast(int, x), C, 0xf, 0xf, true)); }
template <int C> __device__ __forceinline__ unsigned dppu(unsigned x) { return (unsigned)__builtin_amdgcn_mov_dpp((int)x, C, 0xf, 0xf, true); }
constexpr int QX1 = 0xB1, QX2 = 0x4E, HMIR = 0x141, ROR4 = 0x124, ROR8 = 0x128;
__device__ __forceinline__ float x16_sum(float x) { auto s = __builtin_amdgcn_permlane16_swap(__float_as_uint(x), __float_as_uint(x), false, false); return __uint_as_float(s[0]) + __uint_as_float(s[1]); }
__device__ __forceinline__ float x32_sum(float x) { auto s = __builtin_amdgcn_permlane32_swap(__float_as_uint(x), __float_as_uint(x), false, false); return __uint_as_float(s[0]) + __uint_as_float(s[1]); }
__device__ __forceinline__ float x16_max(float x) { auto s = __builtin_amdgcn_permlane16_swap(__float_as_uint(x), __float_as_uint(x), false, false); return fmaxf(__uint_as_float(s[0]), __uint_as_float(s[1])); }
__device__ __forceinline__ float x32_max(float x) { auto s = __builtin_amdgcn_permlane32_swap(__float_as_uint(x), __float_as_uint(x), false, false); return fmaxf(__uint_as_float(s[0]), __uint_as_float(s[1])); }
__device__ __forceinline__ float sum8(float x) { x += dppf<QX1>(x); x += dppf<QX2>(x); x += dppf<HMIR>(x); return x; }
__device__ __forceinline__ float max8(float x) { x = fmaxf(x, dppf<QX1>(x)); x = fmaxf(x, dppf<QX2>(x)); x = fmaxf(x, dppf<HMIR>(x)); return x; }
__device__ __forceinline__ float sum64(float x) { x = sum8(x); x += dppf<ROR8>(x); return x32_sum(x16_sum(x)); }
__device__ __forceinline__ bool okv(v4u v) { return (v.x != SENT) & (v.y != SENT) & (v.z != SENT) & (v.w != SENT); }
#define LGKM0() asm volatile("s_waitcnt lgkmcnt(0)" ::: "memory")
#define VM0() asm volatile("s_waitcnt vmcnt(0)" ::: "memory")
#define VMN(n) asm volatile("s_waitcnt vmcnt(%0)" :: "n"(n) : "memory")
#define BAR() do { asm volatile("s_waitcnt lgkmcnt(0)" ::: "memory"); __builtin_amdgcn_s_barrier(); asm volatile("" ::: "memory"); } while (0)
#define SB() __builtin_amdgcn_sched_barrier(0)
__device__ __forceinline__ void set_flag(LAS unsigned char* L, int f) { LGKM0(); *(volatile LAS unsigned*)(L + L_FLG + 4 * f) = 1u; }
__device__ __forceinline__ void wait_flag(LAS unsigned char* L, int f) { while (__builtin_amdgcn_readfirstlane(*(volatile LAS unsigned*)(L + L_FLG + 4 * f)) == 0u) __builtin_amdgcn_s_sleep(1); asm volatile("" ::: "memory"); }
__device__ __forceinline__ void wait_flag4(LAS unsigned char* L, int f) {
  for (;;) { v4u v = *(volatile LAS v4u*)(L + L_FLG + 4 * f); if (__builtin_amdgcn_readfirstlane(v.x & v.y & v.z & v.w)) break; __builtin_amdgcn_s_sleep(1); }
  asm volatile("" ::: "memory");
}
#define PIN(x) asm volatile("" : "+v"(x))
__device__ __forceinline__ f32x2 unpk(unsigned u) { return f32x2{bf_lo(u), bf_hi(u)}; }
__device__ __forceinline__ f32x2 rb2(f32x2 v) { return unpk(pk(v.x, v.y)); }
__device__ __forceinline__ unsigned mx4(v4u v) { return max(max(v.x, v.y), max(v.z, v.w)); }
template <int N> __device__ __forceinline__ f32x4 sumv(const v4u (&v)[N]) {
  if constexpr (N == 8) return ((asf(v[0]) + asf(v[1])) + (asf(v[2]) + asf(v[3]))) + ((asf(v[4]) + asf(v[5])) + (asf(v[6]) + asf(v[7])));
  else return ((asf(v[0]) + asf(v[1])) + (asf(v[2]) + asf(v[3]))) + asf(v[4]);
}
template <int N, int STR> __device__ __forceinline__ f32x4 gather(rsrc_t r, int vo) {
  v4u v[N];
#pragma unroll
  for (int i = 0; i < N; ++i) v[i] = ld128<AUX_SC1>(r, vo, i * STR);
  f32x4 s4;
  for (int s = 0; s < SPIN; ++s) {
    s4 = sumv<N>(v);
    if (__builtin_amdgcn_ballot_w64(__builtin_isunordered(s4[0], s4[1]) | __builtin_isunordered(s4[2], s4[3])) == 0ull) break;
    unsigned m[N], a = 0;
#pragma unroll
    for (int i = 0; i < N; ++i) { m[i] = mx4(v[i]); a = max(a, m[i]); }
    if (__builtin_amdgcn_ballot_w64(a == SENT) == 0ull) break;
    asm volatile("" ::: "memory");
#pragma unroll
    for (int i = 0; i < N; ++i) if (m[i] == SENT) v[i] = ld128<AUX_SC1>(r, vo, i * STR);
  }
  return s4;
}
template <int N, int STR, int SLP> __device__ __forceinline__ void gatherg(v4u (&v)[N], rsrc_t r, int vo, int vlast, unsigned tag) {
#pragma unroll
  for (int i = 0; i < N; ++i) v[i] = i < N - 1 ? ld128<AUX_SC1>(r, vo, i * STR) : ld128<AUX_SC1>(r, vlast, 0);
  for (int s = 0; s < SPIN; ++s) {
    unsigned m[N], a = 0;
#pragma unroll
    for (int i = 0; i < N; ++i) { m[i] = (v[i].y ^ tag) | (v[i].w ^ tag); a |= m[i]; }
    if (__builtin_amdgcn_ballot_w64(a != 0u) == 0ull) break;
    asm volatile("" ::: "memory");
    __builtin_amdgcn_s_sleep(SLP);
#pragma unroll
    for (int i = 0; i < N; ++i) if (m[i] != 0u) v[i] = i < N - 1 ? ld128<AUX_SC1>(r, vo, i * STR) : ld128<AUX_SC1>(r, vlast, 0);
  }
}
__device__ __forceinline__ void until(unsigned t) { while ((int)((unsigned)__builtin_amdgcn_s_memrealtime() - t) < 0) __builtin_amdgcn_s_sleep(1); }
constexpr float LOG2E = 1.4426950408889634f;
constexpr int KR = 64 - NL, PRE = RING + NL;
constexpr int slot(int i) { return (i < KR ? i : i - NL) % RING; }
constexpr int issued(int pr) {
  return pr < 0 ? PRE : PRE + (pr < 2 ? 2 * (pr + 1) : 4 + (pr - 1) / 2) + (pr == 31 ? 2 : 0);
}
constexpr int issuedv(int u) {
  if (u < 0) return issued(31);
  const int a = issued(31) + (u + 1) / 2, b = 64 + u + 1 + TV, m = a > b ? a : b;
  return m < 128 ? m : 128;
}
constexpr int last_issue() { int u = 0; while (issuedv(u) < 128) ++u; return u; }
static_assert(issued(31) - 64 <= RING, "ring");
static_assert(PRE >= 64 || NL == 0, "the parked items are issued before q is needed");

#define STAMP(i) do { if ((i) == PROBE_J && ((PROBE_W >> w) & 1)) pt = (unsigned)__builtin_amdgcn_s_memrealtime(); } while (0)
#define STAMP_OUT() do { if (pt) until(tstart + 6000 + 4 * (pt - tstart)); } while (0)

__global__ void __launch_bounds__(NTHR, 2) fused(const u16* __restrict__ x, const u16* __restrict__ kc, const u16* __restrict__ vc, const u16* __restrict__ wq,
                                                 const u16* __restrict__ wo, const u16* __restrict__ w1, const u16* __restrict__ b1, const u16* __restrict__ w2,
                                                 const u16* __restrict__ b2, const u16* __restrict__ cc, const u16* __restrict__ wm, u16* __restrict__ out,
                                                 unsigned char* ws) {
  extern __shared__ __attribute__((aligned(16))) unsigned char smem[];
  LAS unsigned char* L = (LAS unsigned char*)smem;
  const int wg = blockIdx.x, tid = threadIdx.x, lane = tid & 63, w = __builtin_amdgcn_readfirstlane(tid >> 6);
  const int xl = wg & 7, il = wg >> 3;
  const int kvh = 2 * xl + (il >> 4), bb = (il >> 3) & 1, sp = il & 7, pair = bb * HKV + kvh;
  const rsrc_t rw = mkrs(ws, WS_END);
  const int n16 = lane & 15, g4 = lane >> 4;
  const bool is_wo = il < 9;
  const bool is_q = il >= 9 && il < 17;
  const int qs = 8 * xl + il - 9;
  const int ms = (il - 17) * 8 + xl;
  const bool is_mod = il >= 17 && il < 24 && ms < 54, is_s = is_mod && ms < 18, is_sl = is_q || is_mod;
  const bool is_mlp = il >= 24 || (il >= 13 && il < 23);
  const int mk_ = il >= 24 ? il - 24 : il - 5;
  const int mr = 2 * (9 * xl + (((3 - xl) & 7) + 8 + (mk_ >> 1)) % 9) + (mk_ & 1);
  const bool early1 = mk_ < 2 * KE;
  unsigned pt = 0;
  STAMP(0);
  const unsigned tstart = (unsigned)__builtin_amdgcn_s_memrealtime();
  const unsigned tag = *(const __attribute__((address_space(4))) unsigned*)(uintptr_t)(ws + WS_EPOCH) + 1u;
  if (w >= 4) __builtin_amdgcn_s_setprio(3);

  if (w < 4) {
    const rsrc_t rk = mkrs(kc + ((size_t)pair * SK + sp * 1024) * DH, 262144), rv = mkrs(vc + ((size_t)pair * SK + sp * 1024) * DH, 262144);
    const int vk = w * 1024 + (n16 >> 2) * 256 + g4 * 64 + (n16 & 3) * 16, vv = w * 1024 + lane * 16;
    v4u ring[RING];
    int so = 0;
#define ITEM(i) ({ if ((i) == 64) so = 0; asm volatile("" : "+s"(so)); const v4u r_ = (i) < 64 ? ld128<AUX_NT>(rk, vk, so) : ld128<AUX_NT>(rv, vv, so); so += 4096; r_; })
#define PACE(i) do { if ((i) % PSTEP == 0) { const unsigned due_ = tz + (T1 + (i) * TAU) / 10; while ((int)((unsigned)__builtin_amdgcn_s_memrealtime() - due_) < 0) {} } } while (0)
    unsigned tz = tstart;
    BAR();
    const uint64_t kb = (uint64_t)(kc + ((size_t)pair * SK + sp * 1024) * DH);
    const v4u rk4 = {(unsigned)kb, (unsigned)(kb >> 32) & 0xffffu, 262144u, 0x00020000u};
    const unsigned lk = (unsigned)(size_t)(L + L_PK) + w * (NL * 1024);
#pragma unroll
    for (int i = 0; i < PRE; ++i) {
      PACE(i);
      if (i >= TH) asm volatile("s_waitcnt vmcnt(%0)" :: "n"(TH - 1));
      if (i >= KR && i < 64) {
        unsigned keep;
        asm volatile("s_mov_b32 %0, m0\n\ts_mov_b32 m0, %4\n\ts_nop 0\n\tbuffer_load_dwordx4 %1, %2, %3 offen nt lds\n\ts_mov_b32 m0, %0"
                     : "=&s"(keep) : "v"(vk), "s"(rk4), "s"(so), "s"(lk + (i - KR) * 1024) : "memory");
        so += 4096;
      } else ring[slot(i)] = ITEM(i);
      SB();
    }
    STAMP(1);
    wait_flag(L, F_Q);
    STAMP(2);
    { const unsigned tg = __builtin_amdgcn_readfirstlane(*(LAS unsigned*)(L + L_FLG + 4 * F_T0Q));
      if ((int)(tg - tz) < 0) tz = tg; }
    const v4u qf = *(LAS v4u*)(L + L_Q + (n16 >> 3) * 256 + (4 * g4 + (n16 & 3)) * 16);
    const v4u z4 = {0u, 0u, 0u, 0u};
    const v4u q0 = (lane & 4) ? z4 : qf, q1 = (lane & 4) ? qf : z4;
    float sc[8];
#pragma unroll
    for (int pr = 0; pr < 32; ++pr) {
      f32x4 d = {0.f, 0.f, 0.f, 0.f};
      if (2 * pr < KR) {
        d = mfma(ring[slot(2 * pr)], q0, d);
        d = mfma(ring[slot(2 * pr + 1)], q1, d);
      } else {
        if (2 * pr == KR) VMN(issued(KR / 2 - 1) - 64 < 63 ? issued(KR / 2 - 1) - 64 : 63);
        LAS unsigned char* pk_ = L + L_PK + w * (NL * 1024) + (2 * pr - KR) * 1024 + lane * 16;
        d = mfma(*(LAS v4u*)pk_, q0, d);
        d = mfma(*(LAS v4u*)(pk_ + 1024), q1, d);
      }
      SB();
#pragma unroll
      for (int i = issued(pr - 1); i < issued(pr); ++i) {
        PACE(i);
        asm volatile("s_waitcnt vmcnt(%0)" :: "n"(TH - 1));
        ring[slot(i)] = ITEM(i);
      }
      const float s = (dppf<0x00>(d[0]) + dppf<0x55>(d[1])) + (dppf<0xAA>(d[2]) + dppf<0xFF>(d[3]));
      sc[pr >> 2] = (pr & 3) == 0 ? s : ((lane & 3) == (pr & 3) ? s : sc[pr >> 2]);
      asm volatile("" : "+v"(sc[pr >> 2]));
      SB();
    }
    STAMP(3);
    float m = -INFINITY;
#pragma unroll
    for (int a = 0; a < 8; ++a) { sc[a] = rb(rb(sc[a]) * 0.08838834764831845f); m = fmaxf(m, sc[a]); }
    m = x32_max(x16_max(max8(m)));
    float l = 0.f;
    unsigned pp[8], pm[8];
#pragma unroll
    for (int a = 0; a < 8; ++a) { const float p = __builtin_amdgcn_exp2f((sc[a] - m) * LOG2E); l += p; pp[a] = pk(p, p); pm[a] = dppu<HMIR>(pp[a]); }
    l = x32_sum(x16_sum(sum8(l)));
    if ((lane & 55) == 0) *(LAS f32x2*)(L + L_WML + w * 16 + (lane >> 3) * 8) = f32x2{m, l};
    set_flag(L, F_ML0 + w);
    const unsigned hm = (lane & 1) ? 0xffff0000u : 0x0000ffffu;
    const int ci = (lane & 7) >> 1;
    const unsigned mk0 = ci == 0 ? hm : 0u, mk1 = ci == 1 ? hm : 0u, mk2 = ci == 2 ? hm : 0u, mk3 = ci == 3 ? hm : 0u;
    f32x4 acc = {0.f, 0.f, 0.f, 0.f};
    SB();
#pragma unroll
    for (int u = 0; u < 64; ++u) {
      const int a = u >> 3, v0 = (u >> 1) & 3, t0 = u & 1;
      unsigned own, oth;
      switch (v0) {
        case 0: own = dppu<0x00>(pp[a]); oth = dppu<0xFF>(pm[a]); break;
        case 1: own = dppu<0x55>(pp[a]); oth = dppu<0xAA>(pm[a]); break;
        case 2: own = dppu<0xAA>(pp[a]); oth = dppu<0x55>(pm[a]); break;
        default: own = dppu<0xFF>(pp[a]); oth = dppu<0x00>(pm[a]); break;
      }
      const unsigned lo = t0 == 0 ? own : oth, hi = t0 == 0 ? oth : own;
      const v4u bp = {lo & mk0, lo & mk1, hi & mk2, hi & mk3};
      acc = mfma(ring[slot(64 + u)], bp, acc);
      SB();
#pragma unroll
      for (int i = issuedv(u - 1); i < issuedv(u); ++i) {
        PACE(i);
        asm volatile("s_waitcnt vmcnt(%0)" :: "n"(TH - 1));
        ring[slot(i)] = ITEM(i);
      }
      if (u == TRIG) *(volatile LAS unsigned*)(L + L_FLG + 4 * (F_V0 + w)) = 1u;
      SB();
    }
    *(LAS v4u*)(L + L_MRG + w * 1024 + lane * 16) = nos4(acc);
    STAMP(4);
  } else {
    const int hw = w - 4;
    float xv[12];
    const int ab = hw >> 1, lq = lane < 48 ? lane : 47;
    if (!(w & 1)) {
      const v4u s4 = {SENT, SENT, SENT, SENT};
      if (w == 6) {
#pragma unroll
        for (int i = 0; i < 4; ++i) *(LAS v4u*)(L + L_MRG + i * 1024 + lane * 16) = s4;
        if (lane < 32) *(LAS unsigned*)(L + L_FLG + lane * 4) = 0u;
        if (lane < 4) *(LAS unsigned*)(L + L_H1 + 128 + lane * 4) = 0u;
      }
      if (w == 6) {
        if (is_s) {
#pragma unroll
          for (int i = 0; i < 3; ++i) st128(rw, i < 2 || lane < 32 ? WS_MOD + ms * 2560 + i * 1024 + lane * 16 : WS_END, s4);
        }
        if (is_q) {
          const int pq = ((qs >> 1) & 1) * HKV + (qs >> 2);
#pragma unroll
          for (int i = 0; i < 4; ++i) st128(rw, WS_H1 + (pq * 8 + (qs & 1) * 4 + i) * 1024 + lane * 16, s4);
          st128(rw, lane < 4 ? WS_ML + (pq * 8 + (qs & 1) * 4 + lane) * 16 : WS_END, s4);
        }
        if (is_mlp) {
#pragma unroll
          for (int i = 0; i < 5; ++i) st128(rw, i < 4 || lane < 32 ? WS_H3 + mr * 4608 + i * 1024 + lane * 16 : WS_END, s4);
        }
      }
      if (!is_s) BAR();
      if (is_sl || is_wo) {
        const rsrc_t rm = is_q ? mkrs(wq, D * NQ * 2) : is_wo ? mkrs(wo + (size_t)xl * 512 * D, 512 * D * 2) : mkrs(wm, D * 6 * D * 2);
        const int rs_ = is_q ? 8192 : is_wo ? 1152 : 6912, n_ = is_wo ? 32 : 36;
        const int sw = is_wo ? 0 : (((lane >> 4) & 1) | (ab << 1)) << 1;
        const int vm = (ab * 8 + (lane >> 3)) * rs_ + (is_q ? qs : is_wo ? il : ms) * 128 + ((lane & 7) ^ sw) * 16;
        if (TWE && !is_s) until(tstart + TWE / 10);
#pragma clang loop unroll(disable)
        for (int i = 0; i < n_; ++i) dma(rm, L + L_WT + ab * 1024 + i * 2048, vm, i * 16 * rs_);
      } else if (is_mlp && early1) {
        const rsrc_t r1 = mkrs(w1, D * F * 2);
        const int v1 = (ab * 16 + (lane >> 2)) * 9216 + mr * 64 + (lane & 3) * 16;
        if (TWE) until(tstart + TWE / 10);
#pragma clang loop unroll(disable)
        for (int i = 0; i < 18; ++i) dma(r1, L + L_WT + ab * 1024 + i * 2048, v1, i * 32 * 9216);
      }
      if (is_s) BAR();
      VM0();
      set_flag(L, F_D0 + ab);
      STAMP(1);
    } else {
      const rsrc_t rx = mkrs(x, 2304), rc = mkrs(cc, 2304);
      v2u xr[3], cr[3];
#pragma unroll
      for (int i = 0; i < 3; ++i) cr[i] = ld64<0>(rc, is_mod ? ab * 1152 + lq * 8 : 2304, i * 384);
#pragma unroll
      for (int i = 0; i < 3; ++i) xr[i] = ld64<0>(rx, is_q || is_mlp ? ab * 1152 + lq * 8 : 2304, i * 384);
      BAR();
      if (is_mod) {
#pragma unroll
        for (int i = 0; i < 3; ++i) {
          float a[4] = {bf_lo(cr[i].x), bf_hi(cr[i].x), bf_lo(cr[i].y), bf_hi(cr[i].y)};
#pragma unroll
          for (int j = 0; j < 4; ++j) a[j] *= __builtin_amdgcn_rcpf(1.f + __builtin_amdgcn_exp2f(-a[j] * LOG2E));
          *(LAS v2u*)(L + L_ACT + ab * 1152 + i * 384 + lq * 8) = v2u{pk(a[0], a[1]), pk(a[2], a[3])};
        }
      }
#pragma unroll
      for (int i = 0; i < 3; ++i) *(LAS v2u*)(L + L_X + ab * 1152 + i * 384 + lq * 8) = xr[i];
      if (!is_q) set_flag(L, F_ACT0 + ab);
#pragma unroll
      for (int i = 0; i < 3; ++i) { xv[4 * i] = bf_lo(xr[i].x); xv[4 * i + 1] = bf_hi(xr[i].x); xv[4 * i + 2] = bf_lo(xr[i].y); xv[4 * i + 3] = bf_hi(xr[i].y); }
      if (is_q) {
        float s = 0.f;
#pragma unroll
        for (int i = 0; i < 12; ++i) s += xv[i];
        const float mean = sum64(lane < 48 ? s : 0.f) * (1.f / D);
        float q = 0.f;
#pragma unroll
        for (int i = 0; i < 12; ++i) { const float a = xv[i] - mean; q += a * a; }
        const float var = sum64(lane < 48 ? q : 0.f) * (1.f / D);
        const float den = rb(__builtin_sqrtf(rb(rb(var) + 1e-5f))), mb = rb(mean);
#pragma unroll
        for (int i = 0; i < 12; ++i) { xv[i] = rb(rb(xv[i] - mb) / den); PIN(xv[i]); }
      }
    }
    v2u modv = {0u, 0u};
    if (is_sl) {
      wait_flag(L, F_D0);
      wait_flag(L, F_D1);
      v4u a[18];
      LAS unsigned char* pa = L + L_WT + (8 * g4 + (n16 >> 2)) * 128 + ((hw ^ ((n16 >> 3) | ((g4 & 1) << 1))) * 32) + 8 * (lane & 3);
#pragma unroll
      for (int s = 0; s < 18; ++s) a[s] = trd2(pa + 32 * s * 128, pa + (32 * s + 4) * 128);
      if (is_q && (w & 1)) {
        v4u sg[7];
        until(tstart + TSG / 10);
        gatherg<7, 768, 8>(sg, rw, WS_SG + (ab * 288 + lq) * 16, WS_T0, tag);
        STAMP(2);
        *(LAS unsigned*)(L + L_FLG + 4 * F_T0) = sg[6].x;
#pragma unroll
        for (int i = 0; i < 3; ++i) {
          const f32x2 h01 = rb2(f32x2{xv[4 * i], xv[4 * i + 1]} * rb2(unpk(sg[3 + i].x) + 1.f)) + unpk(sg[i].x);
          const f32x2 h23 = rb2(f32x2{xv[4 * i + 2], xv[4 * i + 3]} * rb2(unpk(sg[3 + i].z) + 1.f)) + unpk(sg[i].z);
          *(LAS v2u*)(L + L_ACT + ab * 1152 + i * 384 + lq * 8) = v2u{pk(h01.x, h01.y), pk(h23.x, h23.y)};
        }
        set_flag(L, F_ACT0 + ab);
      }
      wait_flag(L, F_ACT0);
      wait_flag(L, F_ACT1);
      LAS unsigned char* p = L + L_ACT + (lane & 1) * 1152 + 16 * g4;
      v4u bq[18];
#pragma unroll
      for (int s = 0; s < 18; ++s) bq[s] = *(LAS v4u*)(p + 64 * s);
      SB();
      f32x4 d = {0.f, 0.f, 0.f, 0.f};
#pragma unroll
      for (int s = 0; s < 18; ++s) d = mfma(a[s], bq[s], d);
      const unsigned p01 = pk(d[0], d[1]), p23 = pk(d[2], d[3]);
      if (is_q || is_s) {
        const int off = is_q ? WS_QG + (n16 * 1024 + qs * 16 + hw * 4 + g4) * 16 : WS_SG + (n16 * 288 + ms * 16 + hw * 4 + g4) * 16;
        st128(rw, n16 < 2 ? off : WS_END, v4u{p01, tag, p23, tag});
        if (w == 4 && (is_q ? (qs & 3) == 0 : ms == 0)) {
          const unsigned tz = is_q ? *(LAS unsigned*)(L + L_FLG + 4 * F_T0) : tstart;
          st128(rw, lane == 0 ? (is_q ? WS_TQ + (qs >> 2) * 128 : WS_T0) : WS_END, v4u{tz, tag, tz, tag});
        }
        if (w == 4) { if (is_s) { STAMP(2); } else { STAMP(5); } }
      } else modv = v2u{nos(p01), nos(p23)};
    }
    set_flag(L, F_S0 + hw);
    if (w == 6) {
      v4u qg[2];
      until(tstart + TQP / 10);
      gatherg<2, 0, 8>(qg, rw, WS_QG + (bb * 1024 + kvh * 64 + lane) * 16, WS_TQ + kvh * 128, tag);
      *(LAS v2u*)(L + L_Q + lane * 8) = v2u{qg[0].x, qg[0].z};
      *(LAS unsigned*)(L + L_FLG + 4 * F_T0Q) = qg[1].x;
      set_flag(L, F_Q);
      STAMP(2);
    }
    if (is_mod && !is_s) {
      wait_flag(L, F_Q);
      st64(rw, n16 < 2 ? WS_MOD + n16 * 4608 + ((ms - 18) * 64 + hw * 16 + g4 * 4) * 2 : WS_END, modv);
    }
    if (w == 6) {
      if (is_wo) {
        v4u qg[2];
        gatherg<2, 0, 32>(qg, rw, WS_QG + ((kvh ^ 1) * 64 + (lane & 60)) * 16, WS_TQ + kvh * 128, tag);
      }
      if (is_mlp) {
        const unsigned bv = lane < 16 ? ld32<0>(mkrs(b1, F * 2), mr * 64 + lane * 4, 0) : ld32<0>(mkrs(b2, D * 2), (mr % 72) * 16 + (lane & 3) * 4, 0);
        if (lane < 20) *(LAS unsigned*)(L + L_BI + lane * 4) = bv;
        v4u mv[9];
        until(tstart + TMG / 10);
        for (int s = 0; s < SPIN; ++s) {
          asm volatile("" ::: "memory");
          unsigned a = 0;
#pragma unroll
          for (int i = 0; i < 9; ++i) mv[i] = ld128<AUX_SC1>(rw, WS_MOD + lane * 16, i * 1024);
#pragma unroll
          for (int i = 0; i < 9; ++i) a = max(a, mx4(mv[i]));
          if (__builtin_amdgcn_ballot_w64(a == SENT) == 0ull) break;
          __builtin_amdgcn_s_sleep(32);
        }
#pragma unroll
        for (int i = 0; i < 9; ++i) *(LAS v4u*)(L + L_MOD + i * 1024 + lane * 16) = mv[i];
        wait_flag(L, F_ACT0);
        wait_flag(L, F_ACT1);
      }
      wait_flag(L, F_D0);
      set_flag(L, F_RDY);
      STAMP(3);
    }
  }

  if (w == 4) {
    wait_flag4(L, F_ML0);
    const int hd = (lane >> 3) & 1;
    f32x2 ml[4];
#pragma unroll
    for (int i = 0; i < 4; ++i) ml[i] = *(LAS f32x2*)(L + L_WML + i * 16 + hd * 8);
    const float mc = fmaxf(fmaxf(ml[0].x, ml[1].x), fmaxf(ml[2].x, ml[3].x));
    float lc = 0.f;
#pragma unroll
    for (int i = 0; i < 4; ++i) lc += ml[i].y * __builtin_amdgcn_exp2f((ml[i].x - mc) * LOG2E);
    if ((lane & 55) == 0) st64(rw, WS_ML + (pair * 8 + sp) * 16 + hd * 8, v2u{nos(__float_as_uint(mc)), nos(__float_as_uint(lc))});
    v2u r;
    for (int s = 0; s < SPIN; ++s) {
      asm volatile("" ::: "memory");
      __builtin_amdgcn_s_sleep(32);
      r = ld64<AUX_SC1>(rw, WS_ML + (pair * 8 + (lane & 7)) * 16 + hd * 8, 0);
      if (__builtin_amdgcn_ballot_w64(max(r.x, r.y) == SENT) == 0ull) break;
    }
    const float ms_ = __uint_as_float(r.x), M = max8(ms_);
    const float Lt = sum8(__uint_as_float(r.y) * __builtin_amdgcn_exp2f((ms_ - M) * LOG2E));
    const float inv = 1.f / Lt;
    float fw[4];
#pragma unroll
    for (int i = 0; i < 4; ++i) { fw[i] = __builtin_amdgcn_exp2f((ml[i].x - M) * LOG2E) * inv; PIN(fw[i]); }
    int vo = WS_H1 + (pair * 8 + sp) * 1024 + lane * 16;
    PIN(vo);
    STAMP(3);
    wait_flag4(L, F_V0);
    v4u pv[4];
    for (;;) {
#pragma unroll
      for (int i = 0; i < 4; ++i) pv[i] = *(volatile LAS v4u*)(L + L_MRG + i * 1024 + lane * 16);
      if (__builtin_amdgcn_ballot_w64(max(max(mx4(pv[0]), mx4(pv[1])), max(mx4(pv[2]), mx4(pv[3]))) == SENT) == 0ull) break;
    }
    st128(rw, vo, nos4((asf(pv[0]) * fw[0] + asf(pv[1]) * fw[1]) + (asf(pv[2]) * fw[2] + asf(pv[3]) * fw[3])));
    STAMP(6);
  } else if ((w & 1) && w > 4 && is_mlp) {
    const int lw = (w - 5) >> 1;
    const rsrc_t r1 = mkrs(w1, D * F * 2), r2 = mkrs(w2, D * F * 2);
    const int v1 = (lw * 16 + (lane >> 2)) * 9216 + mr * 64 + (lane & 3) * 16;
    const int v2 = mr * 36864 + lw * 1024 + lane * 16;
    wait_flag4(L, F_S0);
    wait_flag4(L, F_V0);
    if (TWM) until((unsigned)__builtin_amdgcn_s_memrealtime() + TWM / 10);
    STAMP(4);
    if (!early1) {
#pragma clang loop unroll(disable)
      for (int i = 0; i < 18; ++i) dma(r1, L + L_WT + lw * 1024 + i * 2048, v1, i * 32 * 9216);
    }
#pragma clang loop unroll(disable)
    for (int i = 0; i < 18; ++i) dma(r2, L + L_W2 + lw * 1024 + i * 2048, v2, i * 2048);
    VM0();
    set_flag(L, F_W6 + lw);
    STAMP(5);
  }
  if (is_wo) {
    wait_flag(L, F_RDY);
    if (w < 4) {
      __builtin_amdgcn_s_sleep(S1);
      const f32x4 o = gather<8, 1024>(rw, WS_H1 + ((w >> 1) * HKV + 2 * xl + (w & 1)) * 8192 + lane * 16);
      *(LAS v2u*)(L + L_O + w * 512 + lane * 8) = v2u{pk(o[0], o[1]), pk(o[2], o[3])};
      BAR();
      STAMP(7);
    } else {
      v4u a[16];
      LAS unsigned char* p = L + L_WT + (w - 4) * 32 + (8 * (n16 >> 2) + 2 * g4) * 128 + 8 * (lane & 3);
#pragma unroll
      for (int s = 0; s < 16; ++s) { const int r0 = (s >> 3) * 256 + (s & 1) * 128 + ((s >> 1) & 3) * 32; a[s] = trd2(p + r0 * 128, p + (r0 + 1) * 128); }
      unsigned pb = (unsigned)(size_t)(L + L_O) + (lane & 1) * 1024 + g4 * 16;
      int vo = n16 < 2 ? WS_H2 + xl * 4608 + n16 * 2304 + (il * 64 + (w - 4) * 16 + g4 * 4) * 4 : WS_END;
      PIN(pb); PIN(vo);
      STAMP(7);
      BAR();
      v4u bq[16];
#pragma unroll
      for (int s = 0; s < 16; ++s) bq[s] = *(LAS v4u*)(size_t)(pb + 64 * s);
      SB();
      f32x4 acc = {0.f, 0.f, 0.f, 0.f};
#pragma unroll
      for (int s = 0; s < 16; ++s) acc = mfma(a[s], bq[s], acc);
      st128(rw, vo, nos4(acc));
      STAMP(8);
    }
  } else if (is_mlp) {
    wait_flag(L, F_RDY);
    wait_flag(L, F_W6);
    wait_flag(L, F_W7);
    const int t0_ = w < 4 ? 5 * w : 4 + 4 * w, nt_ = w < 4 ? 5 : 4;
    unsigned ph[5];
#pragma unroll
    for (int t = 0; t < 5; ++t) { ph[t] = (unsigned)(size_t)(L + L_H1) + ((n16 >> 1) == t ? (lane & 1) * 64 + g4 * 16 : 128); PIN(ph[t]); }
    int v3 = (n16 >> 1) < nt_ ? WS_H3 + mr * 4608 + (lane & 1) * 2304 + ((t0_ + (n16 >> 1)) * 16 + g4 * 4) * 4 : WS_END;
    PIN(v3);
    LAS unsigned char* p2 = L + L_W2 + (8 * g4 + (n16 >> 2)) * 1152 + t0_ * 32 + 8 * (lane & 3);
#define X1_PART() do { \
      const int t_ = w * 64 + lane, t = t_ < 288 ? t_ : 287, tb = t >= 144 ? 1 : 0; \
      const v2u xq = *(LAS v2u*)(L + L_X + t * 8), gq = *(LAS v2u*)(L + L_MOD + tb * 4608 + (t - 144 * tb) * 8); \
      f32x2 x01 = unpk(xq.x), x23 = unpk(xq.y), g01 = unpk(gq.x), g23 = unpk(gq.y); \
      unsigned px = (unsigned)(size_t)(L + L_X1) + t * 16; \
      PIN(x01); PIN(x23); PIN(g01); PIN(g23); PIN(px); \
      const f32x4 s = gather<8, 4608>(rw, WS_H2 + t * 16); \
      const f32x2 r01 = rb2(x01 + rb2(g01 * rb2(f32x2{s[0], s[1]}))), r23 = rb2(x23 + rb2(g23 * rb2(f32x2{s[2], s[3]}))); \
      *(LAS f32x4*)(size_t)px = f32x4{r01.x, r01.y, r23.x, r23.y}; \
    } while (0)
#define W2_PART(NT) do { \
      v4u bq[NT]; \
      _Pragma("unroll") for (int t = 0; t < NT; ++t) bq[t] = *(LAS v4u*)(size_t)ph[t]; \
      SB(); \
      f32x4 acc = {0.f, 0.f, 0.f, 0.f}; \
      _Pragma("unroll") for (int t = 0; t < NT; ++t) acc = mfma(a2[t], bq[t], acc); \
      st128(rw, v3, nos4(acc)); \
    } while (0)
    if (w < 4) {
      const int b_ = w >> 1, hf = w & 1;
      float c1[5], sbv[5];
#pragma unroll
      for (int i = 0; i < 5; ++i) {
        const int k = min(lane + 64 * (5 * hf + i), 575);
        c1[i] = rb(1.f + bf_lo(*(LAS u16*)(L + L_MOD + b_ * 4608 + (1152 + k) * 2)));
        sbv[i] = bf_lo(*(LAS u16*)(L + L_MOD + b_ * 4608 + (576 + k) * 2));
        PIN(c1[i]); PIN(sbv[i]);
      }
      unsigned pr_ = (unsigned)(size_t)(L + L_X1) + b_ * 2304 + lane * 4, pw_ = (unsigned)(size_t)(L + L_H2) + b_ * 1280 + (lane + 320 * hf) * 2;
      PIN(pr_); PIN(pw_);
      X1_PART();
      STAMP(9);
      BAR();
      float xv[9];
#pragma unroll
      for (int i = 0; i < 9; ++i) xv[i] = *(LAS float*)(size_t)(pr_ + 256 * i);
      SB();
      const float pv_ = __builtin_bit_cast(float, __builtin_amdgcn_readfirstlane(__builtin_bit_cast(int, xv[0])));
      float dv[9];
#pragma unroll
      for (int i = 0; i < 9; ++i) dv[i] = xv[i] - pv_;
      float s1 = ((dv[0] + dv[1]) + (dv[2] + dv[3])) + ((dv[4] + dv[5]) + (dv[6] + dv[7])) + dv[8];
      float s2 = (dv[0] * dv[0] + dv[1] * dv[1] + dv[2] * dv[2]) + (dv[3] * dv[3] + dv[4] * dv[4] + dv[5] * dv[5]) + (dv[6] * dv[6] + dv[7] * dv[7] + dv[8] * dv[8]);
      s1 = sum64(s1) * (1.f / D); s2 = sum64(s2) * (1.f / D);
      const float var = fmaxf(s2 - s1 * s1, 0.f), mb = rb(pv_ + s1);
      const float rden = __builtin_amdgcn_rcpf(rb(__builtin_amdgcn_sqrtf(rb(rb(var) + 1e-5f))));
      float own[6];
#pragma unroll
      for (int i = 0; i < 5; ++i) own[i] = (hf && i < 4) ? xv[5 + i] : xv[i];
      own[5] = own[4];
#pragma unroll
      for (int i = 0; i < 3; ++i) {
        const f32x2 cc2 = {c1[2 * i], c1[i < 2 ? 2 * i + 1 : 4]}, ss2 = {sbv[2 * i], sbv[i < 2 ? 2 * i + 1 : 4]};
        const f32x2 h = rb2(rb2(rb2(f32x2{own[2 * i], own[2 * i + 1]} - mb) * rden) * cc2) + ss2;
        const unsigned hp_ = pk(h.x, h.y);
        *(LAS u16*)(size_t)(pw_ + 256 * i) = (u16)hp_;
        if (i < 2) *(LAS u16*)(size_t)(pw_ + 256 * i + 128) = (u16)(hp_ >> 16);
      }
      BAR();
      v4u a2[5];
#pragma unroll
      for (int t = 0; t < 5; ++t) a2[t] = trd2(p2 + 32 * t, p2 + 32 * t + 4 * 1152);
      BAR();
      W2_PART(5);
      STAMP(10);
    } else {
      v4u a1[9], a2[4];
      LAS unsigned char* p1 = L + L_WT + (((lane >> 1) & 1) * 288 + 8 * g4 + (n16 >> 2)) * 64 + (w - 4) * 16 + 8 * (lane & 1);
#pragma unroll
      for (int s = 0; s < 9; ++s) a1[s] = trd2(p1 + 32 * s * 64, p1 + (32 * s + 4) * 64);
#pragma unroll
      for (int t = 0; t < 4; ++t) a2[t] = trd2(p2 + 32 * t, p2 + 32 * t + 4 * 1152);
      const v2u tb1 = *(LAS v2u*)(L + L_BI + ((w - 4) * 8 + (g4 & 1) * 4) * 2);
      f32x2 b01 = unpk(tb1.x), b23 = unpk(tb1.y);
      unsigned pb = (unsigned)(size_t)(L + L_H2) + (lane & 1) * 1280 + ((lane >> 1) & 1) * 576 + 16 * g4;
      unsigned pw_ = (unsigned)(size_t)(L + L_H1) + (lane & 1) * 64 + ((w - 4) * 8 + (g4 & 1) * 4) * 2;
      PIN(b01); PIN(b23); PIN(pb); PIN(pw_);
      STAMP(7);
      if (w == 4) X1_PART();
      BAR();
      BAR();
      {
        v4u bq[9];
#pragma unroll
        for (int s = 0; s < 9; ++s) bq[s] = *(LAS v4u*)(size_t)(pb + 64 * s);
        SB();
        f32x4 d = {0.f, 0.f, 0.f, 0.f};
#pragma unroll
        for (int s = 0; s < 9; ++s) d = mfma(a1[s], bq[s], d);
        float e[4];
#pragma unroll
        for (int i = 0; i < 4; ++i) {
          const unsigned sh = dppu<QX2>(__float_as_uint(d[i]));
          e[i] = d[i] + __uint_as_float(__builtin_amdgcn_permlane32_swap(sh, sh, false, false)[1]);
        }
        f32x2 r01 = rb2(rb2(f32x2{e[0], e[1]}) + b01), r23 = rb2(rb2(f32x2{e[2], e[3]}) + b23);
        r01.x = r01.x > 0.f ? r01.x : 0.f; r01.y = r01.y > 0.f ? r01.y : 0.f; r23.x = r23.x > 0.f ? r23.x : 0.f; r23.y = r23.y > 0.f ? r23.y : 0.f;
        r01 *= r01; r23 *= r23;
        if ((lane & 46) == 0) *(LAS v2u*)(size_t)pw_ = v2u{pk(r01.x, r01.y), pk(r23.x, r23.y)};
      }
      BAR();
      W2_PART(4);
      if (w == 7) {
        const int ob = mr >= 72 ? 1 : 0, oc = (mr - 72 * ob) * 8 + (lane & 1) * 4;
        const v2u tb2 = *(LAS v2u*)(L + L_BI + 64 + (lane & 1) * 8), tgb = *(LAS v2u*)(L + L_MOD + ob * 4608 + (1728 + oc) * 2);
        const f32x4 xo = *(LAS f32x4*)(L + L_X1 + mr * 32 + (lane & 1) * 16);
        f32x2 c01 = unpk(tb2.x), c23 = unpk(tb2.y), g01 = unpk(tgb.x), g23 = unpk(tgb.y), x01 = {xo[0], xo[1]}, x23 = {xo[2], xo[3]};
        int vout = lane < 2 ? mr * 16 + lane * 8 : 2304;
        PIN(c01); PIN(c23); PIN(g01); PIN(g23); PIN(x01); PIN(x23); PIN(vout);
        __builtin_amdgcn_s_sleep(H3SLEEP);
        f32x4 t = gather<5, 32 * 4608>(rw, WS_H3 + (lane >> 1) * 4608 + mr * 32 + (lane & 1) * 16);
        STAMP(10);
#pragma unroll
        for (int i = 0; i < 4; ++i) t[i] += dppf<QX2>(t[i]);
#pragma unroll
        for (int i = 0; i < 4; ++i) t[i] += dppf<ROR4>(t[i]);
#pragma unroll
        for (int i = 0; i < 4; ++i) t[i] += dppf<ROR8>(t[i]);
#pragma unroll
        for (int i = 0; i < 4; ++i) t[i] = x16_sum(t[i]);
#pragma unroll
        for (int i = 0; i < 4; ++i) t[i] = x32_sum(t[i]);
        const f32x2 r01 = x01 + rb2(g01 * rb2(rb2(f32x2{t[0], t[1]}) + c01)), r23 = x23 + rb2(g23 * rb2(rb2(f32x2{t[2], t[3]}) + c23));
        __builtin_amdgcn_raw_buffer_store_b64(v2u{pk(r01.x, r01.y), pk(r23.x, r23.y)}, mkrs(out, 2304), vout, 0, 0);
        STAMP(11);
      } else if (w == 6 && mr == 0 && lane == 0) {
        __hip_atomic_store((GAS unsigned*)ws, tag, __ATOMIC_RELAXED, __HIP_MEMORY_SCOPE_AGENT);
      }
    }
  }
  STAMP_OUT();
}
}

__global__ void tiny_rec(unsigned* p) { if (threadIdx.x == 0) __hip_atomic_store(p, (unsigned)__builtin_amdgcn_s_memrealtime(), __ATOMIC_RELAXED, __HIP_MEMORY_SCOPE_AGENT); }
__global__ void tiny_spin(unsigned* p) { unsigned t = __builtin_amdgcn_readfirstlane(__hip_atomic_load(p, __ATOMIC_RELAXED, __HIP_MEMORY_SCOPE_AGENT)); mk::until(t + 10000); }
extern "C" void kernel_launch(void* const* d_in, const int* in_sizes, int n_in, void* d_out, int out_size, void* d_ws, size_t ws_size, hipStream_t stream) {
  using mk::u16;
  (void)hipFuncSetAttribute((const void*)mk::fused, hipFuncAttributeMaxDynamicSharedMemorySize, mk::L_END);
  mk::fused<<<256, mk::NTHR, mk::L_END, stream>>>((const u16*)d_in[0], (const u16*)d_in[1], (const u16*)d_in[2], (const u16*)d_in[3], (const u16*)d_in[4],
                                                  (const u16*)d_in[6], (const u16*)d_in[7], (const u16*)d_in[8], (const u16*)d_in[9], (const u16*)d_in[10],
                                                  (const u16*)d_in[11], (u16*)d_out, (unsigned char*)d_ws + WSOFF);
}
```

```cpp
#define C3ON 0
#define PROBE_J 0
#define PROBE_W 1
#define CODEPF 35712
#include <hip/hip_runtime.h>
#include <hip/hip_bf16.h>
#include <stdint.h>

namespace mk {
typedef unsigned short u16;
typedef unsigned v4u __attribute__((ext_vector_type(4)));
typedef unsigned v2u __attribute__((ext_vector_type(2)));
typedef float f32x4 __attribute__((ext_vector_type(4)));
typedef float f32x2 __attribute__((ext_vector_type(2)));
typedef short s16x4 __attribute__((ext_vector_type(4)));
typedef __bf16 bf16x2 __attribute__((ext_vector_type(2)));
typedef __bf16 bf16x8 __attribute__((ext_vector_type(8)));
typedef __amdgpu_buffer_rsrc_t rsrc_t;
#define LAS __attribute__((address_space(3)))
#define GAS __attribute__((address_space(1)))

constexpr int D = 576, F = 4608, SK = 8192, DH = 128, HKV = 16, NQ = 4096;
constexpr int NTHR = 512;
constexpr int AUX_NT = 2, AUX_SC1 = 16;
#define RING 56
#define NL 14
#define TH 10
#define TV 10
#define TAU 140
#define T1 2800
#define PSTEP 2
#define TSG 5800
#define TQP 8800
#define TWM 300
#define TP1 1400
#define TP2 2600
#define TOUCH 3
#define C3PF 1
#define THL 7
#define LATE 150
#define LATE2 500
#define NOM1 1030
#define C3AUX 2
#define WAUX 2
#define ICHK 7
#define NSL 24
#define DSL 10
#define S3 3
constexpr unsigned SENT = 0xffffffffu;
constexpr int SPIN = 1 << 16;

constexpr int WS_EPOCH = 0;
constexpr int WS_T0 = 256;
constexpr int WS_MG = 4096;
constexpr int WS_QG = 32768;
constexpr int WS_ML = WS_QG + 32768;
constexpr int WS_H2 = WS_ML + 8192;
constexpr int WS_H1 = WS_H2 + 36864;
constexpr int WS_H3 = WS_H1 + 262144;
constexpr int WS_END = WS_H3 + 72 * 4608;
constexpr int WS_DBG = 4 << 20;

constexpr int L_WT = 0;
constexpr int L_PK = L_WT + 73728;
constexpr int L_ACT = L_PK + NL * 4096;
constexpr int L_Q = L_ACT + 2304;
constexpr int L_MRG = L_Q + 512;
constexpr int L_WML = L_MRG + 4096;
constexpr int L_FLG = L_WML + 64;
constexpr int L_MOD = L_FLG + 256;
constexpr int L_X = L_MOD + 7168;
constexpr int L_X1 = L_X + 2304;
constexpr int L_H2 = L_X1 + 4608 + 256;
constexpr int L_O = L_H2 + 2816;
constexpr int L_H1 = L_O + 2304;
constexpr int L_BI = L_H1 + 256;
constexpr int L_END = L_BI + 192;
static_assert(L_END <= 160 * 1024, "LDS");
enum { F_Q = 0, F_RDY = 1, F_ACT0 = 2, F_ACT1 = 3, F_ML0 = 4  , F_V0 = 8  , F_W0 = 12, F_W1 = 13, F_D0 = 14, F_D1 = 15, F_TMX = 16, F_S0 = 20   };

__device__ __forceinline__ float bf_lo(unsigned u) { return __uint_as_float(u << 16); }
__device__ __forceinline__ float bf_hi(unsigned u) { return __uint_as_float(u & 0xffff0000u); }
__device__ __forceinline__ unsigned pk(float a, float b) { f32x2 v = {a, b}; return __builtin_bit_cast(unsigned, __builtin_convertvector(v, bf16x2)); }
__device__ __forceinline__ float rb(float a) { return __uint_as_float(pk(0.f, a)); }
__device__ __forceinline__ f32x2 unpk(unsigned u) { return f32x2{bf_lo(u), bf_hi(u)}; }
__device__ __forceinline__ f32x2 rb2(f32x2 v) { return f32x2{rb(v.x), rb(v.y)}; }
__device__ __forceinline__ unsigned nos(unsigned u) { return u < 0xfffffffeu ? u : 0xfffffffeu; }
__device__ __forceinline__ v4u nos4(f32x4 v) { return v4u{nos(__float_as_uint(v[0])), nos(__float_as_uint(v[1])), nos(__float_as_uint(v[2])), nos(__float_as_uint(v[3]))}; }
__device__ __forceinline__ f32x4 asf(v4u v) { return __builtin_bit_cast(f32x4, v); }
__device__ __forceinline__ unsigned mx4(v4u v) { return max(max(v.x, v.y), max(v.z, v.w)); }
__device__ __forceinline__ rsrc_t mkrs(const void* p, unsigned bytes) { return __builtin_amdgcn_make_buffer_rsrc((void*)p, 0, (int)bytes, 0x00020000); }
__device__ __forceinline__ v4u mkrs4(const void* p, unsigned bytes) { const uint64_t a = (uint64_t)p; return v4u{(unsigned)a, (unsigned)(a >> 32) & 0xffffu, bytes, 0x00020000u}; }
template <int AUX> __device__ __forceinline__ v4u ld128(rsrc_t r, int v, int s) { return __builtin_amdgcn_raw_buffer_load_b128(r, v, s, AUX); }
template <int AUX> __device__ __forceinline__ v2u ld64(rsrc_t r, int v, int s) { return __builtin_amdgcn_raw_buffer_load_b64(r, v, s, AUX); }
template <int AUX> __device__ __forceinline__ unsigned ld32(rsrc_t r, int v, int s) { return __builtin_amdgcn_raw_buffer_load_b32(r, v, s, AUX); }
__device__ __forceinline__ void st128(rsrc_t r, int v, v4u d) { __builtin_amdgcn_raw_buffer_store_b128(d, r, v, 0, AUX_SC1); }
__device__ __forceinline__ void dma(rsrc_t r, LAS unsigned char* l, int v, int s) { __builtin_amdgcn_raw_ptr_buffer_load_lds(r, (LAS void*)l, 16, v, s, 0, WAUX); }
__device__ __forceinline__ f32x4 mfma(v4u a, v4u b, f32x4 c) { return __builtin_amdgcn_mfma_f32_16x16x32_bf16(__builtin_bit_cast(bf16x8, a), __builtin_bit_cast(bf16x8, b), c, 0, 0, 0); }
__device__ __forceinline__ v2u trd(LAS unsigned char* p) { return __builtin_bit_cast(v2u, __builtin_amdgcn_ds_read_tr16_b64_v4i16((LAS s16x4*)p)); }
__device__ __forceinline__ v4u trd2(LAS unsigned char* p0, LAS unsigned char* p1) { v2u a = trd(p0), b = trd(p1); return v4u{a.x, a.y, b.x, b.y}; }
template <int C> __device__ __forceinline__ float dppf(float x) { return __builtin_bit_cast(float, __builtin_amdgcn_mov_dpp(__builtin_bit_cast(int, x), C, 0xf, 0xf, true)); }
template <int C> __device__ __forceinline__ unsigned dppu(unsigned x) { return (unsigned)__builtin_amdgcn_mov_dpp((int)x, C, 0xf, 0xf, true); }
constexpr int QX1 = 0xB1, QX2 = 0x4E, HMIR = 0x141, ROR4 = 0x124, ROR8 = 0x128;
__device__ __forceinline__ float x16_sum(float x) { auto s = __builtin_amdgcn_permlane16_swap(__float_as_uint(x), __float_as_uint(x), false, false); return __uint_as_float(s[0]) + __uint_as_float(s[1]); }
__device__ __forceinline__ float x32_sum(float x) { auto s = __builtin_amdgcn_permlane32_swap(__float_as_uint(x), __float_as_uint(x), false, false); return __uint_as_float(s[0]) + __uint_as_float(s[1]); }
__device__ __forceinline__ float x16_max(float x) { auto s = __builtin_amdgcn_permlane16_swap(__float_as_uint(x), __float_as_uint(x), false, false); return fmaxf(__uint_as_float(s[0]), __uint_as_float(s[1])); }
__device__ __forceinline__ float x32_max(float x) { auto s = __builtin_amdgcn_permlane32_swap(__float_as_uint(x), __float_as_uint(x), false, false); return fmaxf(__uint_as_float(s[0]), __uint_as_float(s[1])); }
__device__ __forceinline__ float sum8(float x) { x += dppf<QX1>(x); x += dppf<QX2>(x); x += dppf<HMIR>(x); return x; }
__device__ __forceinline__ float max8(float x) { x = fmaxf(x, dppf<QX1>(x)); x = fmaxf(x, dppf<QX2>(x)); x = fmaxf(x, dppf<HMIR>(x)); return x; }
__device__ __forceinline__ float sum64(float x) { x = sum8(x); x += dppf<ROR8>(x); return x32_sum(x16_sum(x)); }
#define LGKM0() asm volatile("s_waitcnt lgkmcnt(0)" ::: "memory")
#define VM0() asm volatile("s_waitcnt vmcnt(0)" ::: "memory")
#define VMN(n) asm volatile("s_waitcnt vmcnt(%0)" :: "n"(n) : "memory")
#define BAR() do { asm volatile("s_waitcnt lgkmcnt(0)" ::: "memory"); __builtin_amdgcn_s_barrier(); asm volatile("" ::: "memory"); } while (0)
#define SB() __builtin_amdgcn_sched_barrier(0)
#define PIN(x) asm volatile("" : "+v"(x))
__device__ __forceinline__ void set_flag(LAS unsigned char* L, int f) { LGKM0(); *(volatile LAS unsigned*)(L + L_FLG + 4 * f) = 1u; }
__device__ __forceinline__ void wait_flag(LAS unsigned char* L, int f) {
  while (__builtin_amdgcn_readfirstlane(*(volatile LAS unsigned*)(L + L_FLG + 4 * f)) == 0u) __builtin_amdgcn_s_sleep(1);
  asm volatile("" ::: "memory");
}
__device__ __forceinline__ void wait_flag2(LAS unsigned char* L, int f) {
  for (;;) { v2u v = *(volatile LAS v2u*)(L + L_FLG + 4 * f); if (__builtin_amdgcn_readfirstlane(v.x & v.y)) break; __builtin_amdgcn_s_sleep(1); }
  asm volatile("" ::: "memory");
}
__device__ __forceinline__ void wait_flag4(LAS unsigned char* L, int f) {
  for (;;) { v4u v = *(volatile LAS v4u*)(L + L_FLG + 4 * f); if (__builtin_amdgcn_readfirstlane(v.x & v.y & v.z & v.w)) break; __builtin_amdgcn_s_sleep(1); }
  asm volatile("" ::: "memory");
}
__device__ __forceinline__ void until(unsigned t) { while ((int)((unsigned)__builtin_amdgcn_s_memrealtime() - t) < 0) __builtin_amdgcn_s_sleep(1); }

__device__ __forceinline__ v4u tag4(unsigned e) { return v4u{e & 1023u, (e + (e >> 10)) & 1023u, (e + (e >> 20)) & 1023u, (e + (e >> 10) + (e >> 22)) & 1023u}; }
__device__ __forceinline__ v4u enc4(f32x4 v, v4u t) { return (__builtin_bit_cast(v4u, v) & ~1023u) | t; }

__device__ __forceinline__ unsigned or4(v4u y) { return (y.x | y.y) | (y.z | y.w); }
#define G_EACH(M) M(0) M(1) M(2) M(3) M(4) if constexpr (N == 8) { M(5) M(6) M(7) }
template <int N, int STR, class MID> __device__ __forceinline__ f32x4 gather(rsrc_t r, int vo, v4u t, int lane, MID mid) {
  v4u y0, y1, y2, y3, y4, y5, y6, y7;
  unsigned m0, m1, m2, m3, m4, m5, m6, m7, a = 0;
#define G_LD(i) y##i = ld128<AUX_SC1>(r, vo, i * STR); SB();
#define G_CHK(i) y##i ^= t; if (N == 5 && i == 4) y##i = lane < 32 ? y##i : v4u{0u, 0u, 0u, 0u}; m##i = or4(y##i); a |= m##i;
#define G_RLD(i) if (m##i & 1023u) y##i = ld128<AUX_SC1>(r, vo, i * STR);
#define G_RCK(i) if (m##i & 1023u) { y##i ^= t; m##i = or4(y##i); a |= m##i; }
  G_EACH(G_LD)
  mid();
  SB();
  f32x4 sum;
  G_CHK(0) SB(); G_CHK(1) f32x4 p01 = asf(y0) + asf(y1); PIN(p01); SB(); G_CHK(2) SB(); G_CHK(3) f32x4 p03 = p01 + (asf(y2) + asf(y3)); PIN(p03); PIN(a); SB(); G_CHK(4)
  if constexpr (N == 8) { SB(); G_CHK(5) f32x4 p45 = asf(y4) + asf(y5); PIN(p45); SB(); G_CHK(6) PIN(a); SB(); G_CHK(7) sum = p03 + (p45 + (asf(y6) + asf(y7))); }
  else sum = p03 + asf(y4);
  PIN(sum);
  if (__builtin_amdgcn_ballot_w64((a & 1023u) != 0u) != 0ull) {
    for (int s = 0; s < SPIN; ++s) {
      asm volatile("" ::: "memory");
      G_EACH(G_RLD)
      a = 0;
      G_EACH(G_RCK)
      if (__builtin_amdgcn_ballot_w64((a & 1023u) != 0u) == 0ull) break;
    }
    sum = (asf(y0) + asf(y1)) + (asf(y2) + asf(y3));
    if constexpr (N == 8) sum += (asf(y4) + asf(y5)) + (asf(y6) + asf(y7)); else sum += asf(y4);
  }
  return sum;
}
template <int N, int STR, int SLP> __device__ __forceinline__ void gatherg(v4u (&v)[N], rsrc_t r, int vo, int vlast, unsigned tag) {
#pragma unroll
  for (int i = 0; i < N; ++i) v[i] = i < N - 1 ? ld128<AUX_SC1>(r, vo, i * STR) : ld128<AUX_SC1>(r, vlast, 0);
  for (int s = 0; s < SPIN; ++s) {
    unsigned m[N], a = 0;
#pragma unroll
    for (int i = 0; i < N; ++i) { m[i] = (v[i].y ^ tag) | (v[i].w ^ tag); a |= m[i]; }
    if (__builtin_amdgcn_ballot_w64(a != 0u) == 0ull) break;
    asm volatile("" ::: "memory");
    __builtin_amdgcn_s_sleep(SLP);
#pragma unroll
    for (int i = 0; i < N; ++i) if (m[i] != 0u) v[i] = i < N - 1 ? ld128<AUX_SC1>(r, vo, i * STR) : ld128<AUX_SC1>(r, vlast, 0);
  }
}
template <int NS, bool TR> __device__ __forceinline__ f32x4 gemv(const v4u (&a)[18], unsigned pb) {
  v4u bq[NS];
#pragma unroll
  for (int s = 0; s < NS; ++s) bq[s] = *(LAS v4u*)(size_t)(pb + 64 * s);
  SB();
  f32x4 d = {0.f, 0.f, 0.f, 0.f};
#pragma unroll
  for (int s = 0; s < NS; ++s) d = TR ? mfma(bq[s], a[s], d) : mfma(a[s], bq[s], d);
  return d;
}
template <int NH> __device__ __forceinline__ f32x2 gemv2(const v4u (&a)[18], unsigned pb) {
  v4u bq[NH];
#pragma unroll
  for (int s = 0; s < NH; ++s) bq[s] = *(LAS v4u*)(size_t)(pb + 64 * s);
  SB();
  f32x4 d0 = {0.f, 0.f, 0.f, 0.f}, d1 = {0.f, 0.f, 0.f, 0.f};
#pragma unroll
  for (int s = 0; s < NH; ++s) { d0 = mfma(bq[s], a[s], d0); d1 = mfma(bq[s], a[NH + s], d1); }
  return f32x2{d0[0], d0[1]} + f32x2{d1[2], d1[3]};
}
constexpr float LOG2E = 1.4426950408889634f;

constexpr int KR = 64 - NL, PRE = RING + NL;
constexpr int slot(int i) { return (i < KR ? i : i - NL) % RING; }
constexpr int issued(int pr) {
  return pr < 0 ? PRE : PRE + (pr < 2 ? 2 * (pr + 1) : 4 + (pr - 1) / 2);
}
constexpr int ISM = issued(31) + 3;
constexpr int issuedv(int u) {
  if (u < 0) return ISM;
  const int a = ISM + (u + 1) / 2, b = 64 + u + 1 + TV, m = a > b ? a : b;
  return m < 128 ? m : 128;
}
constexpr int last_issue() { int u = 0; while (issuedv(u) < 128) ++u; return u; }
static_assert(ISM - 64 <= RING && (PRE >= 64 || NL == 0) && NL % 2 == 0, "ring");

#define STAMP(i) do { if ((i) == PROBE_J && ((PROBE_W >> w) & 1)) pt = (unsigned)__builtin_amdgcn_s_memrealtime(); } while (0)
#define STAMP_OUT() do { if (pt) until(tstart + 6000 + 1000 * (((unsigned)(uintptr_t)wq >> 7) & 31)); } while (0)

#define CS(i)

__global__ void __launch_bounds__(NTHR, 2) fused(const u16* __restrict__ x, const u16* __restrict__ kc, const u16* __restrict__ vc, const u16* __restrict__ wq,
                                                 const u16* __restrict__ wo, const u16* __restrict__ w1, const u16* __restrict__ b1, const u16* __restrict__ w2,
                                                 const u16* __restrict__ b2, const u16* __restrict__ cc, const u16* __restrict__ wm, u16* __restrict__ out,
                                                 unsigned char* ws) {
  extern __shared__ __attribute__((aligned(16))) unsigned char smem[];
  LAS unsigned char* L = (LAS unsigned char*)smem;
  const int wg = blockIdx.x, tid = threadIdx.x, lane = tid & 63, w = __builtin_amdgcn_readfirstlane(tid >> 6);
  const int xl = wg & 7, il = wg >> 3;
  const int n16 = lane & 15, g4 = lane >> 4;
  const bool is_wo = il < 9;
  const bool is_mlp = il >= 9 && il < 18;
  const bool is_q = il >= 18 && il < 26;
  const int mr = 9 * xl + il - 9, qs = 8 * xl + il - 18, ms = (il - 26) * 8 + xl;
  const bool is_mod = il >= 26 && ms < 45;
  unsigned pt = 0;
  STAMP(0);
  v4u cpf0, cpf1;
  if (w == 1) {
    const rsrc_t rc = mkrs((const void*)(__builtin_amdgcn_s_getpc() & ~255ull), CODEPF);
    cpf0 = ld128<0>(rc, il * 1024 + lane * 16, 0);
    cpf1 = ld128<0>(rc, il * 1024 + lane * 16, 32768);
  }
  const unsigned long long tstart64 = __builtin_amdgcn_s_memrealtime();
  const unsigned tstart = (unsigned)tstart64;
  const int ab = (w >> 1) & 1;
  int rs_ = is_wo ? 1152 : is_mlp ? 9216 : is_q ? 8192 : 6912;
  int vm = (is_wo ? 512 * xl + 8 * ((lane >> 3) & 3) + (lane >> 5) + 2 * ab : 8 * ab + (lane >> 3)) * rs_
           + (is_wo ? il : is_mlp ? mr : is_q ? qs : ms) * 128 + ((lane & 7) ^ ((((lane >> 4) & 1) | (ab << 1)) << 1)) * 16;
  int nblk = is_wo ? 32 : (il < 26 || is_mod) ? 36 : 0;
  PIN(vm);
  asm volatile("" : "+s"(rs_), "+s"(nblk));
  if (w == 0) {
    const v4u s4 = {SENT, SENT, SENT, SENT};
#pragma unroll
    for (int i = 0; i < 4; ++i) *(LAS v4u*)(L + L_MRG + i * 1024 + lane * 16) = s4;
    *(LAS unsigned*)(L + L_FLG + lane * 4) = 0u;
  }
  asm volatile("" :: "s"(x), "s"(kc), "s"(vc), "s"(wq), "s"(wo), "s"(w1), "s"(b1), "s"(w2), "s"(b2), "s"(cc), "s"(wm), "s"(out), "s"(ws), "s"(tstart64));
  BAR();
  const int kvh = 2 * xl + (il >> 4), bb = (il >> 3) & 1, sp = il & 7, pair = bb * HKV + kvh;
  const rsrc_t rm = mkrs(is_wo ? wo : is_mlp ? w1 : is_q ? wq : wm, (is_wo ? NQ : D) * rs_);
  unsigned probe = 0;
  if (w < 4) probe = ld32<WAUX>(rm, ((is_wo ? 512 * xl : 0) + (lane & 3) * 170) * rs_ + (is_wo ? il : is_mlp ? mr : is_q ? qs : ms) * 128, 0);
  if ((w & 5) == 4) {
    __builtin_amdgcn_s_setprio(3);
    STAMP(7);
#pragma clang loop unroll(disable)
    for (int i = 0; i < nblk; ++i) {
      const int r = is_wo ? 4 * (i & 1) + 64 * (i & 2) + 8 * (i & 12) + 16 * (i & 16) : 16 * i;
      dma(rm, L + L_WT + ab * 1024 + i * 2048, vm, r * rs_);
    }
  }
  const rsrc_t rw = mkrs(ws, WS_END);
#define LOAD_TAG() ({ const unsigned char* p_ = ws; asm volatile("" : "+s"(p_)); *(const __attribute__((address_space(4))) unsigned*)(uintptr_t)(p_ + WS_EPOCH) + 1u; })
#define UNTIL_ALL(T) do { until(tstart + (T) / 10); until(tstart + (T) / 10 + __builtin_amdgcn_readfirstlane(*(volatile LAS unsigned*)(L + L_FLG + 4 * F_TMX))); } while (0)
#define X1_PART(MID) do { \
      const int t_ = w * 64 + lane, t = t_ < 288 ? t_ : 287; \
      f32x2 x0 = unpk(*(LAS unsigned*)(L + L_X + t * 4)), x1_ = unpk(*(LAS unsigned*)(L + L_X + 1152 + t * 4)); \
      f32x2 g0 = unpk(*(LAS unsigned*)(L + L_MOD + t * 4)), g1 = unpk(*(LAS unsigned*)(L + L_MOD + 3584 + t * 4)); \
      unsigned px = (unsigned)(size_t)(L + L_X1) + t * 8; \
      PIN(x0); PIN(x1_); PIN(g0); PIN(g1); PIN(px); \
      wait_flag2(L, F_W0);        \
      until(tl + TP2 / 10); \
      CS(10); \
      const f32x4 s = gather<8, 4608>(rw, WS_H2 + t * 16, t4, lane, MID); \
      CS(1); \
      *(LAS f32x2*)(size_t)px = rb2(x0 + rb2(g0 * rb2(f32x2{s[0], s[2]}))); \
      *(LAS f32x2*)(size_t)(px + 2304) = rb2(x1_ + rb2(g1 * rb2(f32x2{s[1], s[3]}))); \
    } while (0)

  if (w < 4) {
    unsigned c3a, c3b;
    {
      const u16* m_ = w == 2 ? wq : w1;
      const int c0 = (3 - ((unsigned)(uintptr_t)m_ >> 7) - (w == 2 ? 8 : 9) * xl) & 7, st_ = w == 2 ? 8192 : 9216;
      const int v_ = lane < 48 ? (il * 24 + (lane >> 1)) * st_ + ((w == 2 ? 8 : 9) * xl + c0) * 128 + (lane & 1) * 64 : 0x40000000;
      const unsigned na = w >= 2 ? D * st_ : 0, nb = w == 3 && c0 == 0 ? D * st_ : 0;
      c3a = ld32<C3AUX>(mkrs(m_, C3ON ? na : 0), v_, 0);
      c3b = ld32<C3AUX>(mkrs(m_, C3ON ? nb : 0), v_, 1024);
    }
    asm volatile("" :: "v"(probe));
    const unsigned tpr = (unsigned)__builtin_amdgcn_s_memrealtime() - NOM1 / 10;
    STAMP(6);
    const unsigned tag = LOAD_TAG();
    if (w == 1) asm volatile("" :: "v"(cpf0), "v"(cpf1));
    STAMP(5);
    unsigned tz = tpr;
    if (w == 0 && il >= 8 && il < 10) st128(rw, lane == 0 ? WS_T0 + (wg & 15) * 16 : WS_END, v4u{tz, tag, tz, tag});
    v4u tu;
    bool tknown = false;
#define T0_ASK() do { if (!tknown) tu = ld128<AUX_SC1>(rw, WS_T0 + n16 * 16, 0); } while (0)
#define T0_USE() do { if (!tknown && __builtin_amdgcn_ballot_w64(((tu.y ^ tag) | (tu.w ^ tag)) != 0u) == 0ull) { \
      tknown = true; \
      const float d_ = fminf(fmaxf((float)(int)(tu.x - tstart), -500.f), 500.f); \
      float mx_ = max8(d_), sm_ = sum8(d_); \
      mx_ = fmaxf(mx_, dppf<ROR8>(mx_)); sm_ += dppf<ROR8>(sm_); \
      int o_ = (int)(sm_ * 0.0625f); \
      PIN(o_); \
      tz = tstart + __builtin_amdgcn_readfirstlane(o_); \
      if (w == 0) *(volatile LAS unsigned*)(L + L_FLG + 4 * F_TMX) = (unsigned)(int)fmaxf(mx_, 0.f); \
    } } while (0)
    const rsrc_t rk = mkrs(kc + ((size_t)pair * SK + sp * 1024) * DH, 262144), rv = mkrs(vc + ((size_t)pair * SK + sp * 1024) * DH, 262144);
    const v4u rk4 = mkrs4(kc + ((size_t)pair * SK + sp * 1024) * DH, 262144);
    const int vk = w * 1024 + (n16 >> 2) * 256 + g4 * 64 + (n16 & 3) * 16, vv = w * 1024 + lane * 16;
    v4u ring[RING];
    int so = 0;
#define ITEM(i) ({ if ((i) == 64) so = 0; asm volatile("" : "+s"(so)); const v4u r_ = (i) < 64 ? ld128<AUX_NT>(rk, vk, so) : ld128<AUX_NT>(rv, vv, so); so += 4096; r_; })
#define DUE(i) (T1 + (i) * TAU + ((i) > 128 - NSL ? ((i) - (128 - NSL)) * DSL : 0))
#define PACE(i) do { if ((i) % 2 == 0) asm volatile("1:\n\ts_memrealtime vcc\n\ts_waitcnt lgkmcnt(0)\n\ts_sub_i32 vcc_lo, vcc_lo, %0\n\ts_cmpk_lt_i32 vcc_lo, %1\n\t" \
                       "s_cbranch_scc1 1b\n\ts_cmpk_gt_i32 vcc_lo, %3\n\ts_cbranch_scc1 3f\n\ts_cmpk_gt_i32 vcc_lo, %2\n\ts_cbranch_scc1 2f\n\ts_waitcnt vmcnt(%4)\n2:\n\ts_waitcnt vmcnt(%5)\n3:\n\ts_waitcnt vmcnt(%6)" \
                       :: "s"(tz), "n"(DUE(i) / 10), "n"((DUE(i) + LATE) / 10), "n"((DUE(i) + LATE2) / 10), "n"(THL - 2), "n"(THL - 1), "n"(TH - 2) : "vcc", "scc"); } while (0)
    const unsigned lk = (unsigned)(size_t)(L + L_PK) + w * (NL * 1024);
#pragma unroll
    for (int i = 0; i < PRE; ++i) {
      PACE(i);
      if (i >= KR && i < 64) {
        unsigned keep;
        asm volatile("s_mov_b32 %0, m0\n\ts_mov_b32 m0, %4\n\ts_nop 0\n\tbuffer_load_dwordx4 %1, %2, %3 offen nt lds\n\ts_mov_b32 m0, %0"
                     : "=&s"(keep) : "v"(vk), "s"(rk4), "s"(so), "s"(lk + (i - KR) * 1024) : "memory");
        so += 4096;
      } else ring[slot(i)] = ITEM(i);
      if (i == 12) asm volatile("" :: "v"(c3a), "v"(c3b));
      if (i == ICHK + 10 || i == ICHK + 22 || i == ICHK + 40) T0_USE();
      if (i == ICHK || i == ICHK + 12 || i == ICHK + 30) T0_ASK();
      SB();
    }
    STAMP(1);
    wait_flag(L, F_Q);
    STAMP(2);
    const v4u qf = *(LAS v4u*)(L + L_Q + (n16 >> 3) * 256 + (4 * g4 + (n16 & 3)) * 16);
    const v4u z4 = {0u, 0u, 0u, 0u};
    const v4u q0 = (lane & 4) ? z4 : qf, q1 = (lane & 4) ? qf : z4;
    float sc[8];
#pragma unroll
    for (int pr = 0; pr < 32; ++pr) {
      f32x4 d = {0.f, 0.f, 0.f, 0.f};
      if (2 * pr < KR) {
        d = mfma(ring[slot(2 * pr)], q0, d);
        d = mfma(ring[slot(2 * pr + 1)], q1, d);
      } else {
        if (2 * pr == KR) VMN(issued(KR / 2 - 1) - 64 < 63 ? issued(KR / 2 - 1) - 64 : 63);
        LAS unsigned char* pk_ = L + L_PK + w * (NL * 1024) + (2 * pr - KR) * 1024 + lane * 16;
        d = mfma(*(LAS v4u*)pk_, q0, d);
        d = mfma(*(LAS v4u*)(pk_ + 1024), q1, d);
      }
      SB();
#pragma unroll
      for (int i = issued(pr - 1); i < issued(pr); ++i) { PACE(i); ring[slot(i)] = ITEM(i); }
      const float s = (dppf<0x00>(d[0]) + dppf<0x55>(d[1])) + (dppf<0xAA>(d[2]) + dppf<0xFF>(d[3]));
      sc[pr >> 2] = (pr & 3) == 0 ? s : ((lane & 3) == (pr & 3) ? s : sc[pr >> 2]);
      PIN(sc[pr >> 2]);
      SB();
    }
    STAMP(3);
#define ISSUE1(i) do { PIN(m); SB(); PACE(i); ring[slot(i)] = ITEM(i); SB(); } while (0)
    float m = -INFINITY;
#pragma unroll
    for (int a = 0; a < 8; ++a) { sc[a] = rb(rb(sc[a]) * 0.08838834764831845f); m = fmaxf(m, sc[a]); }
    ISSUE1(ISM - 3);
    m = x32_max(x16_max(max8(m)));
    ISSUE1(ISM - 2);
    float l = 0.f;
    unsigned pp[8], pm[8];
#pragma unroll
    for (int a = 0; a < 8; ++a) { const float p = __builtin_amdgcn_exp2f((sc[a] - m) * LOG2E); l += p; pp[a] = pk(p, p); pm[a] = dppu<HMIR>(pp[a]); }
    PIN(l);
    ISSUE1(ISM - 1);
    l = x32_sum(x16_sum(sum8(l)));
    if ((lane & 55) == 0) *(LAS f32x2*)(L + L_WML + w * 16 + (lane >> 3) * 8) = f32x2{m, l};
    set_flag(L, F_ML0 + w);
    const unsigned hm = (lane & 1) ? 0xffff0000u : 0x0000ffffu;
    const int ci = (lane & 7) >> 1;
    const unsigned mk0 = ci == 0 ? hm : 0u, mk1 = ci == 1 ? hm : 0u, mk2 = ci == 2 ? hm : 0u, mk3 = ci == 3 ? hm : 0u;
    f32x4 acc = {0.f, 0.f, 0.f, 0.f};
    unsigned tl = 0;
    SB();
#pragma unroll
    for (int u = 0; u < 64; ++u) {
      const int a = u >> 3, v0 = (u >> 1) & 3, t0 = u & 1;
      unsigned own, oth;
      switch (v0) {
        case 0: own = dppu<0x00>(pp[a]); oth = dppu<0xFF>(pm[a]); break;
        case 1: own = dppu<0x55>(pp[a]); oth = dppu<0xAA>(pm[a]); break;
        case 2: own = dppu<0xAA>(pp[a]); oth = dppu<0x55>(pm[a]); break;
        default: own = dppu<0xFF>(pp[a]); oth = dppu<0x00>(pm[a]); break;
      }
      const unsigned lo = t0 == 0 ? own : oth, hi = t0 == 0 ? oth : own;
      const v4u bp = {lo & mk0, lo & mk1, hi & mk2, hi & mk3};
      acc = mfma(ring[slot(64 + u)], bp, acc);
      SB();
#pragma unroll
      for (int i = issuedv(u - 1); i < issuedv(u); ++i) { PACE(i); ring[slot(i)] = ITEM(i); }
      if (u == last_issue()) { tl = (unsigned)__builtin_amdgcn_s_memrealtime() | 1u; *(volatile LAS unsigned*)(L + L_FLG + 4 * (F_V0 + w)) = tl; }
      SB();
    }
    *(LAS v4u*)(L + L_MRG + w * 1024 + lane * 16) = nos4(acc);
    STAMP(4);
    const v4u t4 = tag4(tag);
    if (is_wo) {
      unsigned po = (unsigned)(size_t)(L + L_O) + w * 576 + lane * 8;
      PIN(po);
      until(tl + TP1 / 10);
      const f32x4 o = gather<8, 1024>(rw, WS_H1 + ((w >> 1) * HKV + 2 * xl + (w & 1)) * 8192 + lane * 16, t4, lane, [] {});
      *(LAS v2u*)(size_t)po = v2u{pk(o[0], o[1]), pk(o[2], o[3])};
      BAR();
      STAMP(7);
    } else if (is_mlp) {
      wait_flag(L, F_RDY);
      const int b_ = w >> 1, hf = w & 1;
      f32x2 c1[3], sbv[3];
#pragma unroll
      for (int i = 0; i < 3; ++i) {
        const int k = lane + 64 * (i < 2 ? 2 * hf + i : 4);
        c1[i] = rb2(unpk(*(LAS unsigned*)(L + L_MOD + b_ * 3584 + 2304 + k * 4)) + 1.f);
        sbv[i] = unpk(*(LAS unsigned*)(L + L_MOD + b_ * 3584 + 1152 + k * 4));
        PIN(c1[i]); PIN(sbv[i]);
      }
      unsigned pr_ = (unsigned)(size_t)(L + L_X1) + b_ * 2304 + lane * 8, pw_ = (unsigned)(size_t)(L + L_H2) + b_ * 1408 + lane * 4;
      unsigned pw2 = pw_ + 512 * hf;
      PIN(pr_); PIN(pw_); PIN(pw2);
      LAS unsigned char* p2 = L + L_WT + (8 * g4 + (n16 >> 2)) * 1152 + (16 + hf) * 64 + 8 * (lane & 3);
      unsigned ph = (unsigned)(size_t)(L + L_H1) + (lane & 1) * 128 + g4 * 16;
      int v3 = WS_H3 + mr * 4608 + (16 + hf) * 256 + lane * 16;
      v4u tv = t4, a0, a1_, a2, a3;
      PIN(ph); PIN(v3); PIN(tv);
      auto frags = [&] { a0 = trd2(p2, p2 + 4 * 1152); a1_ = trd2(p2 + 32 * 1152, p2 + 36 * 1152); a2 = trd2(p2 + 32, p2 + 32 + 4 * 1152); a3 = trd2(p2 + 32 + 32 * 1152, p2 + 32 + 36 * 1152); };
      X1_PART(frags);
      STAMP(9);
      CS(2);
      BAR();
      CS(3);
      f32x2 xv[5];
#pragma unroll
      for (int i = 0; i < 5; ++i) xv[i] = *(LAS f32x2*)(size_t)(pr_ + 512 * i);
      SB();
      const float pv_ = __builtin_bit_cast(float, __builtin_amdgcn_readfirstlane(__builtin_bit_cast(int, xv[0].x)));
      f32x2 dv[5];
#pragma unroll
      for (int i = 0; i < 5; ++i) dv[i] = xv[i] - pv_;
      dv[4] = lane < 32 ? dv[4] : f32x2{0.f, 0.f};
      const f32x2 a1 = ((dv[0] + dv[1]) + (dv[2] + dv[3])) + dv[4];
      const f32x2 a2_ = (dv[0] * dv[0] + dv[1] * dv[1]) + (dv[2] * dv[2] + dv[3] * dv[3] + dv[4] * dv[4]);
      const auto sw_ = __builtin_amdgcn_permlane32_swap(__float_as_uint(a1.x + a1.y), __float_as_uint(a2_.x + a2_.y), false, false);
      float r_ = sum8(__uint_as_float(sw_[0]) + __uint_as_float(sw_[1]));
      r_ += dppf<ROR8>(r_);
      r_ = x16_sum(r_) * (1.f / D);
      const auto s12 = __builtin_amdgcn_permlane32_swap(__float_as_uint(r_), __float_as_uint(r_), false, false);
      const float s1 = __uint_as_float(s12[0]), s2 = __uint_as_float(s12[1]);
      const f32x2 mv_ = rb2(f32x2{pv_ + s1, fmaxf(s2 - s1 * s1, 0.f)});
      const float rden = __builtin_amdgcn_rcpf(rb(__builtin_amdgcn_sqrtf(rb(mv_.y + 1e-5f))));
      f32x2 h[3];
#define STAGE(E) do { _Pragma("unroll") for (int i = 0; i < 3; ++i) h[i] = E; asm volatile("" : "+v"(h[0]), "+v"(h[1]), "+v"(h[2])); } while (0)
      STAGE(rb2((i == 2 ? xv[4] : hf ? xv[2 + i] : xv[i]) - mv_.x));
      STAGE(rb2(h[i] * rden));
      STAGE(rb2(h[i] * c1[i]));
      STAGE(h[i] + sbv[i]);
#pragma unroll
      for (int i = 0; i < 3; ++i) *(LAS unsigned*)(size_t)(i == 2 ? pw_ + 1024 : pw2 + 256 * i) = pk(h[i].x, h[i].y);
      CS(4);
      BAR();
      CS(5);
      if (w < 2) {
        BAR();
        const v4u bq0 = *(LAS v4u*)(size_t)ph, bq1 = *(LAS v4u*)(size_t)(ph + 64);
        SB();
        const f32x4 z_ = {0.f, 0.f, 0.f, 0.f};
        const f32x4 e0 = mfma(bq1, a1_, mfma(bq0, a0, z_)), e1 = mfma(bq1, a3, mfma(bq0, a2, z_));
        if (lane < 16) st128(rw, v3, enc4(f32x4{e0[0], e0[1], e1[0], e1[1]}, tv));
        CS(8);
      } else BAR();
    }
  } else {
    const int hw = w - 4, lq = lane < 48 ? lane : 47;
    float xv[12];
    unsigned tag;
    if (!(w & 1)) {
      tag = LOAD_TAG();
      VM0();
      set_flag(L, F_D0 + ab);
      STAMP(1);
    } else {
      __builtin_amdgcn_s_setprio(3);
#define STOUCH(P) do { const u16* p_ = (P); asm volatile("" : "+s"(p_)); const unsigned t_ = *(const __attribute__((address_space(4))) unsigned*)(uintptr_t)p_; asm volatile("" :: "s"(t_)); } while (0)
      if ((TOUCH & 1) && il < 2) { STOUCH(kc + (size_t)(il * HKV + 2 * xl + ab) * SK * DH); STAMP(3); }
      v2u xr[3];
      const rsrc_t rx = mkrs(is_mod ? cc : x, 2304);
#pragma unroll
      for (int i = 0; i < 3; ++i) xr[i] = ld64<0>(rx, ab * 1152 + lq * 8, i * 384);
      tag = LOAD_TAG();
#pragma unroll
      for (int i = 0; i < 3; ++i) { xv[4 * i] = bf_lo(xr[i].x); xv[4 * i + 1] = bf_hi(xr[i].x); xv[4 * i + 2] = bf_lo(xr[i].y); xv[4 * i + 3] = bf_hi(xr[i].y); }
      if (is_mod) {
#pragma unroll
        for (int i = 0; i < 12; ++i) xv[i] *= __builtin_amdgcn_rcpf(1.f + __builtin_amdgcn_exp2f(-xv[i] * LOG2E));
#pragma unroll
        for (int i = 0; i < 3; ++i) *(LAS v2u*)(L + L_ACT + ab * 1152 + i * 384 + lq * 8) = v2u{pk(xv[4 * i], xv[4 * i + 1]), pk(xv[4 * i + 2], xv[4 * i + 3])};
      } else if (is_q) {
        float s = 0.f;
#pragma unroll
        for (int i = 0; i < 12; ++i) s += xv[i];
        const float mean = sum64(lane < 48 ? s : 0.f) * (1.f / D);
        float q = 0.f;
#pragma unroll
        for (int i = 0; i < 12; ++i) { const float a = xv[i] - mean; q += a * a; }
        const float var = sum64(lane < 48 ? q : 0.f) * (1.f / D);
        const float den = rb(__builtin_sqrtf(rb(rb(var) + 1e-5f))), mb = rb(mean);
#pragma unroll
        for (int i = 0; i < 12; ++i) { xv[i] = rb(rb(xv[i] - mb) / den); PIN(xv[i]); }
      } else {
#pragma unroll
        for (int i = 0; i < 3; ++i) *(LAS v2u*)(L + L_X + ab * 1152 + i * 384 + lq * 8) = xr[i];
      }
      if (!is_q) set_flag(L, F_ACT0 + ab);
    }
    v4u a[18];
    LAS unsigned char* pa = L + L_WT + (8 * g4 + (n16 >> 2)) * 128 + ((hw ^ ((n16 >> 3) | ((g4 & 1) << 1))) * 32) + 8 * (lane & 3);
    wait_flag2(L, F_D0);
#pragma unroll
    for (int s = 0; s < 18; ++s) a[s] = trd2(pa + 32 * s * 128, pa + (32 * s + 4) * 128);
    set_flag(L, F_S0 + hw);
    const unsigned pact = (unsigned)(size_t)(L + L_ACT) + (lane & 1) * 1152 + 16 * g4;
    if (is_q || is_mod) {
      if (is_q && (w & 1)) {
        v4u sg[6];
        UNTIL_ALL(TSG);
        gatherg<6, 768, 8>(sg, rw, WS_MG + (ab * 864 + lq) * 16, WS_MG + (ab * 864 + lq) * 16 + 5 * 768, tag);
        STAMP(2);
#pragma unroll
        for (int i = 0; i < 3; ++i) {
          const f32x2 h01 = rb2(f32x2{xv[4 * i], xv[4 * i + 1]} * rb2(unpk(sg[3 + i].x) + 1.f)) + unpk(sg[i].x);
          const f32x2 h23 = rb2(f32x2{xv[4 * i + 2], xv[4 * i + 3]} * rb2(unpk(sg[3 + i].z) + 1.f)) + unpk(sg[i].z);
          *(LAS v2u*)(L + L_ACT + ab * 1152 + i * 384 + lq * 8) = v2u{pk(h01.x, h01.y), pk(h23.x, h23.y)};
        }
        set_flag(L, F_ACT0 + ab);
      }
      wait_flag2(L, F_ACT0);
      const f32x4 d = gemv<18, false>(a, pact);
      const int off = is_q ? WS_QG + (n16 * 1024 + qs * 16 + hw * 4 + g4) * 16 : WS_MG + (n16 * 864 + ms * 16 + hw * 4 + g4) * 16;
      st128(rw, n16 < 2 ? off : WS_END, v4u{pk(d[0], d[1]), tag, pk(d[2], d[3]), tag});
      if (w == 4) { if (is_q) { STAMP(5); } else { STAMP(2); } }
    }
    const v4u t4 = tag4(tag);
    const bool is_gb = is_mod && ms < 9;
    if ((TOUCH & 2) && (w & 1) && il < 4) {
      until(tstart + 650);
      STOUCH(il < 2 ? vc + (size_t)(il * HKV + 2 * xl + ab) * SK * DH : il == 2 ? w2 + (size_t)(9 * xl + ab * 8) * 64 * D : ab ? out : b2);
    }
    if (w == 6) {
      v4u qg[1];
      UNTIL_ALL(TQP);
      gatherg<1, 0, 8>(qg, rw, 0, WS_QG + (bb * 1024 + kvh * 64 + lane) * 16, tag);
      *(LAS v2u*)(L + L_Q + lane * 8) = v2u{qg[0].x, qg[0].z};
      wait_flag2(L, F_ACT0);
      set_flag(L, F_Q);
      STAMP(2);
      if (is_mlp) {
        const unsigned bv = lane < 32 ? ld32<0>(mkrs(b1, F * 2), mr * 128 + lane * 4, 0) : ld32<0>(mkrs(b2, D * 2), (((mr >> 2) * 2 + ((lane >> 1) & 1)) * 16 + (mr & 3) * 4) * 2 + (lane & 1) * 4, 0);
        if (lane < 36) *(LAS unsigned*)(L + L_BI + lane * 4) = bv;
#pragma unroll
        for (int b_ = 0; b_ < 2; ++b_) {
          v4u mv[7];
          gatherg<7, 1024, 32>(mv, rw, WS_MG + (b_ * 864 + 288 + lane) * 16, WS_MG + (b_ * 864 + 288 + 384 + lq) * 16, tag);
#pragma unroll
          for (int i = 0; i < 7; ++i) *(LAS v2u*)(L + L_MOD + b_ * 3584 + i * 512 + lane * 8) = v2u{mv[i].x, mv[i].z};
        }
        set_flag(L, F_RDY);
        STAMP(3);
      }
    } else if (w == 4) {
      wait_flag4(L, F_ML0);
      const int hd = (lane >> 3) & 1;
      f32x2 ml[4];
#pragma unroll
      for (int i = 0; i < 4; ++i) ml[i] = *(LAS f32x2*)(L + L_WML + i * 16 + hd * 8);
      const float mc = fmaxf(fmaxf(ml[0].x, ml[1].x), fmaxf(ml[2].x, ml[3].x));
      float lc = 0.f;
#pragma unroll
      for (int i = 0; i < 4; ++i) lc += ml[i].y * __builtin_amdgcn_exp2f((ml[i].x - mc) * LOG2E);
      st128(rw, (lane & 55) == 0 ? WS_ML + (pair * 8 + sp) * 32 + hd * 16 : WS_END, v4u{__float_as_uint(mc), tag, __float_as_uint(lc), tag});
      v4u r[1];
      __builtin_amdgcn_s_sleep(48);
      gatherg<1, 0, 24>(r, rw, 0, WS_ML + (pair * 8 + (lane & 7)) * 32 + hd * 16, tag);
      const float ms_ = __uint_as_float(r[0].x), M = max8(ms_);
      const float Lt = sum8(__uint_as_float(r[0].z) * __builtin_amdgcn_exp2f((ms_ - M) * LOG2E));
      const float inv = 1.f / Lt;
      float fw[4];
#pragma unroll
      for (int i = 0; i < 4; ++i) { fw[i] = __builtin_amdgcn_exp2f((ml[i].x - M) * LOG2E) * inv; PIN(fw[i]); }
      int vo = WS_H1 + (pair * 8 + sp) * 1024 + lane * 16;
      v4u tv = t4;
      PIN(vo); PIN(tv);
      STAMP(3);
      wait_flag4(L, F_V0);
      v4u pv[4];
      for (;;) {
#pragma unroll
        for (int i = 0; i < 4; ++i) pv[i] = *(volatile LAS v4u*)(L + L_MRG + i * 1024 + lane * 16);
        if (__builtin_amdgcn_ballot_w64(max(max(mx4(pv[0]), mx4(pv[1])), max(mx4(pv[2]), mx4(pv[3]))) == SENT) == 0ull) break;
      }
      st128(rw, vo, enc4((asf(pv[0]) * fw[0] + asf(pv[1]) * fw[1]) + (asf(pv[2]) * fw[2] + asf(pv[3]) * fw[3]), tv));
      STAMP(6);
    } else if (is_mlp || is_gb) {
      const rsrc_t r2 = is_mlp ? mkrs(w2, D * F * 2) : mkrs(wm, D * 6912);
      const int v2 = is_mlp ? mr * 73728 + ab * 1024 + lane * 16 : vm + 45 * 128, st2 = is_mlp ? 2048 : 16 * 6912;
      wait_flag4(L, F_S0);
      wait_flag4(L, F_V0);
      if (TWM) until((unsigned)__builtin_amdgcn_s_memrealtime() + TWM / 10);
      STAMP(4);
#pragma clang loop unroll(disable)
      for (int i = 0; i < 36; ++i) dma(r2, L + L_WT + ab * 1024 + i * 2048, v2, i * st2);
      VM0();
      set_flag(L, F_W0 + ab);
      STAMP(5);
    }
    if (is_wo) {
      unsigned pb = (unsigned)(size_t)(L + L_O) + (lane & 1) * 1152 + ((lane >> 1) & 1) * 576 + g4 * 16;
      int vo = lane < 16 ? WS_H2 + xl * 4608 + (il * 64 + hw * 16 + lane) * 8 : WS_END;
      unsigned ta = (lane & 1) ? t4.z : t4.x, tb = (lane & 1) ? t4.w : t4.y;
      PIN(pb); PIN(vo); PIN(ta); PIN(tb);
      BAR();
      const f32x2 d = gemv2<8>(a, pb);
      __builtin_amdgcn_raw_buffer_store_b64(v2u{(__float_as_uint(d[0]) & ~1023u) | ta, (__float_as_uint(d[1]) & ~1023u) | tb}, rw, vo, 0, AUX_SC1);
      STAMP(8);
    } else if (is_mlp) {
      wait_flag(L, F_RDY);
      const unsigned tl = __builtin_amdgcn_readfirstlane(*(LAS unsigned*)(L + L_FLG + 4 * F_V0));
      unsigned ph = (unsigned)(size_t)(L + L_H1) + (lane & 1) * 128 + g4 * 16, mkv = ~1023u;
      int v3 = WS_H3 + mr * 4608 + hw * 1024 + lane * 16;
      v4u tv = t4;
      PIN(v3); PIN(ph); PIN(tv); PIN(mkv);
      const v4u rw4 = mkrs4(ws, WS_END);
      v4u a2[16];
      LAS unsigned char* p2 = L + L_WT + (8 * g4 + (n16 >> 2)) * 1152 + hw * 256 + 8 * (lane & 3);
      float b1v = bf_lo(*(LAS u16*)(L + L_BI + (hw * 16 + n16) * 2));
      unsigned pb = (unsigned)(size_t)(L + L_H2) + (lane & 1) * 1408 + ((lane >> 1) & 1) * 576 + 16 * g4;
      unsigned pw_ = (unsigned)(size_t)(L + L_H1) + (hw * 16 + n16) * 2;
      PIN(b1v); PIN(pb); PIN(pw_);
      __builtin_amdgcn_s_setprio(0);
      const int oj = (lane >> 2) & 3, oc = ((mr >> 2) * 2 + (oj >> 1)) * 16 + (mr & 3) * 4 + (lane & 3);
      auto w2frags = [&] {
#pragma unroll
        for (int t = 0; t < 8; ++t) { a2[2 * t] = trd2(p2 + 32 * t, p2 + 32 * t + 4 * 1152); a2[2 * t + 1] = trd2(p2 + 32 * t + 32 * 1152, p2 + 32 * t + 36 * 1152); }
      };
      if (w == 4) X1_PART(w2frags);
      else { wait_flag2(L, F_W0); w2frags(); }
      BAR();
      const int vg = w == 5 ? WS_MG + ((oj & 1) * 864 + 720 + (oc >> 2)) * 16 : WS_END;
      v4u g = ld128<AUX_SC1>(rw, vg, 0);
      float b2v = bf_lo(*(LAS u16*)(L + L_BI + 128 + ((oj >> 1) * 4 + (lane & 3)) * 2)), xo = *(LAS float*)(L + L_X1 + ((oj & 1) * 576 + oc) * 4);
      int vout = lane < 16 ? ((oj & 1) * 576 + oc) * 2 : 2304, vh3 = WS_H3 + (lane >> 2) * 4608 + mr * 64 + (lane & 3) * 16;
      PIN(b2v); PIN(xo); PIN(vout); PIN(vh3);
      BAR();
      CS(5);
      {
        f32x2 r = rb2(rb2(gemv2<9>(a, pb)) + b1v);
        r.x = r.x > 0.f ? r.x : 0.f; r.y = r.y > 0.f ? r.y : 0.f;
        r *= r;
        const unsigned hp_ = pk(r.x, r.y);
        *(LAS u16*)(size_t)pw_ = (u16)hp_;
        *(LAS u16*)(size_t)(pw_ + 128) = (u16)(hp_ >> 16);
      }
      PIN(g);
      CS(6);
      BAR();
      CS(7);
      {
        const v4u bq0 = *(LAS v4u*)(size_t)ph, bq1 = *(LAS v4u*)(size_t)(ph + 64);
        SB();
        asm volatile(
            "s_nop 1\n\t"
            "v_mfma_f32_16x16x32_bf16 v[176:179], %[b0], %[a0], 0\n\t"
            "v_mfma_f32_16x16x32_bf16 v[180:183], %[b0], %[a2], 0\n\t"
            "v_mfma_f32_16x16x32_bf16 v[176:179], %[b1], %[a1], v[176:179]\n\t"
            "v_mfma_f32_16x16x32_bf16 v[180:183], %[b1], %[a3], v[180:183]\n\t"
            "v_mfma_f32_16x16x32_bf16 v[184:187], %[b0], %[a4], 0\n\t"
            "v_mfma_f32_16x16x32_bf16 v[188:191], %[b0], %[a6], 0\n\t"
            "v_mfma_f32_16x16x32_bf16 v[184:187], %[b1], %[a5], v[184:187]\n\t"
            "v_mfma_f32_16x16x32_bf16 v[188:191], %[b1], %[a7], v[188:191]\n\t"
            "v_mfma_f32_16x16x32_bf16 v[192:195], %[b0], %[a8], 0\n\t"
            "v_mfma_f32_16x16x32_bf16 v[196:199], %[b0], %[a10], 0\n\t"
            "v_mfma_f32_16x16x32_bf16 v[192:195], %[b1], %[a9], v[192:195]\n\t"
            "v_mfma_f32_16x16x32_bf16 v[196:199], %[b1], %[a11], v[196:199]\n\t"
            "v_mfma_f32_16x16x32_bf16 v[200:203], %[b0], %[a12], 0\n\t"
            "v_and_b32_dpp v208, v176, %[mk] quad_perm:[0,1,2,3] row_mask:0x1 bank_mask:0xf\n\t"
            "v_mfma_f32_16x16x32_bf16 v[204:207], %[b0], %[a14], 0\n\t"
            "v_and_b32_dpp v209, v177, %[mk] quad_perm:[0,1,2,3] row_mask:0x1 bank_mask:0xf\n\t"
            "v_mfma_f32_16x16x32_bf16 v[200:203], %[b1], %[a13], v[200:203]\n\t"
            "v_and_b32_dpp v210, v180, %[mk] quad_perm:[0,1,2,3] row_mask:0x1 bank_mask:0xf\n\t"
            "v_mfma_f32_16x16x32_bf16 v[204:207], %[b1], %[a15], v[204:207]\n\t"
            "v_and_b32_dpp v211, v181, %[mk] quad_perm:[0,1,2,3] row_mask:0x1 bank_mask:0xf\n\t"
            "v_and_b32_dpp v208, v184, %[mk] quad_perm:[0,1,2,3] row_mask:0x2 bank_mask:0xf\n\t"
            "v_and_b32_dpp v209, v185, %[mk] quad_perm:[0,1,2,3] row_mask:0x2 bank_mask:0xf\n\t"
            "v_and_b32_dpp v210, v188, %[mk] quad_perm:[0,1,2,3] row_mask:0x2 bank_mask:0xf\n\t"
            "v_and_b32_dpp v211, v189, %[mk] quad_perm:[0,1,2,3] row_mask:0x2 bank_mask:0xf\n\t"
            "v_and_b32_dpp v208, v192, %[mk] quad_perm:[0,1,2,3] row_mask:0x4 bank_mask:0xf\n\t"
            "v_and_b32_dpp v209, v193, %[mk] quad_perm:[0,1,2,3] row_mask:0x4 bank_mask:0xf\n\t"
            "v_and_b32_dpp v210, v196, %[mk] quad_perm:[0,1,2,3] row_mask:0x4 bank_mask:0xf\n\t"
            "v_and_b32_dpp v211, v197, %[mk] quad_perm:[0,1,2,3] row_mask:0x4 bank_mask:0xf\n\t"
            "v_and_b32_dpp v208, v200, %[mk] quad_perm:[0,1,2,3] row_mask:0x8 bank_mask:0xf\n\t"
            "v_and_b32_dpp v209, v201, %[mk] quad_perm:[0,1,2,3] row_mask:0x8 bank_mask:0xf\n\t"
            "v_and_b32_dpp v210, v204, %[mk] quad_perm:[0,1,2,3] row_mask:0x8 bank_mask:0xf\n\t"
            "v_and_b32_dpp v211, v205, %[mk] quad_perm:[0,1,2,3] row_mask:0x8 bank_mask:0xf\n\t"
            "v_or_b32 v208, v208, v216\n\t"
            "v_or_b32 v209, v209, v217\n\t"
            "v_or_b32 v210, v210, v218\n\t"
            "v_or_b32 v211, v211, v219\n\t"
            "buffer_store_dwordx4 v[208:211], %[vo], %[rs], 0 offen sc1\n\t"
            "s_nop 1"
            :: [b0] "v"(bq0), [b1] "v"(bq1), [a0] "v"(a2[0]), [a1] "v"(a2[1]), [a2] "v"(a2[2]), [a3] "v"(a2[3]), [a4] "v"(a2[4]), [a5] "v"(a2[5]), [a6] "v"(a2[6]), [a7] "v"(a2[7]), [a8] "v"(a2[8]), [a9] "v"(a2[9]), [a10] "v"(a2[10]), [a11] "v"(a2[11]), [a12] "v"(a2[12]), [a13] "v"(a2[13]), [a14] "v"(a2[14]), [a15] "v"(a2[15]),
               [vo] "v"(v3), [rs] "s"(rw4), [mk] "v"(mkv), "{v[216:219]}"(tv)
            : "v176", "v177", "v178", "v179", "v180", "v181", "v182", "v183", "v184", "v185", "v186", "v187", "v188", "v189", "v190", "v191", "v192", "v193", "v194", "v195", "v196", "v197", "v198", "v199", "v200", "v201", "v202", "v203", "v204", "v205", "v206", "v207", "v208", "v209", "v210", "v211", "memory");
        STAMP(9);
        CS(8);
      }
      if (w == 5) {
        if (__builtin_amdgcn_ballot_w64(((g.y ^ tag) | (g.w ^ tag)) != 0u) != 0ull) {
          v4u g1[1];
          gatherg<1, 0, 1>(g1, rw, 0, vg, tag);
          g = g1[0];
        }
        const unsigned gp = (lane & 2) ? g.z : g.x;
        float gbv = (lane & 1) ? bf_hi(gp) : bf_lo(gp);
        PIN(gbv);
        __builtin_amdgcn_s_sleep(S3);
        CS(2);
        f32x4 t = gather<5, 16 * 4608>(rw, vh3, t4, lane, [] {});
        STAMP(10);
        CS(9);
#pragma unroll
        for (int i = 0; i < 4; ++i) t[i] += dppf<ROR4>(t[i]);
#pragma unroll
        for (int i = 0; i < 4; ++i) t[i] += dppf<ROR8>(t[i]);
        float u = (oj & 2) ? ((oj & 1) ? t[3] : t[2]) : ((oj & 1) ? t[1] : t[0]);
        u = x32_sum(x16_sum(u));
        __builtin_amdgcn_raw_buffer_store_b16((u16)pk(xo + rb(gbv * rb(rb(u) + b2v)), 0.f), mkrs(out, 2304), vout, 0, 0);
        STAMP(11);
        CS(11);
      } else if (w == 4 && mr == 0 && lane == 0) {
        __hip_atomic_store((GAS unsigned*)ws, tag, __ATOMIC_RELAXED, __HIP_MEMORY_SCOPE_AGENT);
      }
    } else if (is_gb) {
      wait_flag2(L, F_W0);
#pragma unroll
      for (int s = 0; s < 18; ++s) a[s] = trd2(pa + 32 * s * 128, pa + (32 * s + 4) * 128);
      const f32x4 d = gemv<18, false>(a, pact);
      st128(rw, n16 < 2 ? WS_MG + (n16 * 864 + (45 + ms) * 16 + hw * 4 + g4) * 16 : WS_END, v4u{pk(d[0], d[1]), tag, pk(d[2], d[3]), tag});
      STAMP(8);
    }
  }
  STAMP_OUT();
}
}

extern "C" void kernel_launch(void* const* d_in, const int* in_sizes, int n_in, void* d_out, int out_size, void* d_ws, size_t ws_size, hipStream_t stream) {
  using mk::u16;
  (void)hipFuncSetAttribute((const void*)mk::fused, hipFuncAttributeMaxDynamicSharedMemorySize, mk::L_END);
  mk::fused<<<256, mk::NTHR, mk::L_END, stream>>>((const u16*)d_in[0], (const u16*)d_in[1], (const u16*)d_in[2], (const u16*)d_in[3], (const u16*)d_in[4],
                                                  (const u16*)d_in[6], (const u16*)d_in[7], (const u16*)d_in[8], (const u16*)d_in[9], (const u16*)d_in[10],
                                                  (const u16*)d_in[11], (u16*)d_out, (unsigned char*)d_ws);
}
```

```cpp
#include <hip/hip_runtime.h>
#include <hip/hip_bf16.h>
#include <stdint.h>

namespace mk {
typedef unsigned short u16;
typedef unsigned v4u __attribute__((ext_vector_type(4)));
typedef unsigned v2u __attribute__((ext_vector_type(2)));
typedef float f32x4 __attribute__((ext_vector_type(4)));
typedef float f32x2 __attribute__((ext_vector_type(2)));
typedef short s16x4 __attribute__((ext_vector_type(4)));
typedef __bf16 bf16x2 __attribute__((ext_vector_type(2)));
typedef __bf16 bf16x8 __attribute__((ext_vector_type(8)));
typedef __amdgpu_buffer_rsrc_t rsrc_t;
#define LAS __attribute__((address_space(3)))
#define GAS __attribute__((address_space(1)))

constexpr int D = 576, F = 4608, SK = 8192, DH = 128, HKV = 16, NQ = 4096;
constexpr int NTHR = 512;
constexpr int AUX_NT = 2, AUX_SC1 = 16;
constexpr int RING = 56, NL = 14;
constexpr int T1 = 2800, TAU = 138;
constexpr int NSL = 24, DSL = 12;
constexpr int THL = 7, TH = 10, TV = 10;
constexpr int LATE = 150, LATE2 = 500;
constexpr int BW = 11;
constexpr int NOM1 = 1030, ICHK = 7;
constexpr float BETA = 0.15f, BETH = 0.4f;
constexpr int CK0 = 40, CK1 = 80, CKD = 18, CAPL = 1000, DEAD = 70, LX = 500;
constexpr float KX = 1.5f;
constexpr int TSG = 5800, TQP = 8800;
constexpr int TWM = 400;
constexpr int TP1 = 1410, TP2 = (TP1 + 1100), TP3 = (TP2 + 1700);
constexpr int CODEPF = 38144;
constexpr unsigned SENT = 0xffffffffu;
constexpr int SPIN = 1 << 19;

constexpr int WS_EPOCH = 0;
constexpr int WS_T0 = 256;
constexpr int WS_LT = 1024;
constexpr int WS_MG = 4096;
constexpr int WS_QG = 32768;
constexpr int WS_ML = WS_QG + 32768;
constexpr int WS_H2 = WS_ML + 8192;
constexpr int WS_H1 = WS_H2 + 36864;
constexpr int WS_H3 = WS_H1 + 262144;
constexpr int WS_END = WS_H3 + 72 * 4608;

constexpr int L_WT = 0;
constexpr int L_PK = L_WT + 73728;
constexpr int L_ACT = L_PK + NL * 4096;
constexpr int L_Q = L_ACT + 2304;
constexpr int L_MRG = L_Q + 512;
constexpr int L_WML = L_MRG + 4096;
constexpr int L_FLG = L_WML + 64;
constexpr int L_MOD = L_FLG + 256;
constexpr int L_X = L_MOD + 7168;
constexpr int L_X1 = L_X + 2304;
constexpr int L_H2 = L_X1 + 4608 + 256;
constexpr int L_O = L_H2 + 2816;
constexpr int L_H1 = L_O + 2304;
constexpr int L_BI = L_H1 + 256;
constexpr int L_END = L_BI + 192;
static_assert(L_END <= 160 * 1024, "LDS");
enum { F_Q = 0, F_RDY = 1, F_ACT0 = 2, F_ACT1 = 3, F_ML0 = 4  , F_V0 = 8  , F_W0 = 12, F_W1 = 13, F_D0 = 14, F_D1 = 15, F_TMX = 16, F_TZ = 17, F_LT0 = 18, F_LT1 = 19, F_H1 = 26, F_I0 = 28, F_I1 = 29, F_SH0 = 24, F_SH1 = 25, F_S0 = 20   };

__device__ __forceinline__ float bf_lo(unsigned u) { return __uint_as_float(u << 16); }
__device__ __forceinline__ float bf_hi(unsigned u) { return __uint_as_float(u & 0xffff0000u); }
__device__ __forceinline__ unsigned pk(float a, float b) { f32x2 v = {a, b}; return __builtin_bit_cast(unsigned, __builtin_convertvector(v, bf16x2)); }
__device__ __forceinline__ float rb(float a) { return __uint_as_float(pk(0.f, a)); }
__device__ __forceinline__ f32x2 unpk(unsigned u) { return f32x2{bf_lo(u), bf_hi(u)}; }
__device__ __forceinline__ f32x2 rb2(f32x2 v) { return f32x2{rb(v.x), rb(v.y)}; }
__device__ __forceinline__ unsigned nos(unsigned u) { return u < 0xfffffffeu ? u : 0xfffffffeu; }
__device__ __forceinline__ v4u nos4(f32x4 v) { return v4u{nos(__float_as_uint(v[0])), nos(__float_as_uint(v[1])), nos(__float_as_uint(v[2])), nos(__float_as_uint(v[3]))}; }
__device__ __forceinline__ f32x4 asf(v4u v) { return __builtin_bit_cast(f32x4, v); }
__device__ __forceinline__ unsigned mx4(v4u v) { return max(max(v.x, v.y), max(v.z, v.w)); }
__device__ __forceinline__ rsrc_t mkrs(const void* p, unsigned bytes) { return __builtin_amdgcn_make_buffer_rsrc((void*)p, 0, (int)bytes, 0x00020000); }
__device__ __forceinline__ v4u mkrs4(const void* p, unsigned bytes) { const uint64_t a = (uint64_t)p; return v4u{(unsigned)a, (unsigned)(a >> 32) & 0xffffu, bytes, 0x00020000u}; }
template <int AUX> __device__ __forceinline__ v4u ld128(rsrc_t r, int v, int s) { return __builtin_amdgcn_raw_buffer_load_b128(r, v, s, AUX); }
template <int AUX> __device__ __forceinline__ v2u ld64(rsrc_t r, int v, int s) { return __builtin_amdgcn_raw_buffer_load_b64(r, v, s, AUX); }
template <int AUX> __device__ __forceinline__ unsigned ld32(rsrc_t r, int v, int s) { return __builtin_amdgcn_raw_buffer_load_b32(r, v, s, AUX); }
__device__ __forceinline__ void st128(rsrc_t r, int v, v4u d) { __builtin_amdgcn_raw_buffer_store_b128(d, r, v, 0, AUX_SC1); }
__device__ __forceinline__ void dma(rsrc_t r, LAS unsigned char* l, int v, int s) { __builtin_amdgcn_raw_ptr_buffer_load_lds(r, (LAS void*)l, 16, v, s, 0, AUX_NT); }
__device__ __forceinline__ f32x4 mfma(v4u a, v4u b, f32x4 c) { return __builtin_amdgcn_mfma_f32_16x16x32_bf16(__builtin_bit_cast(bf16x8, a), __builtin_bit_cast(bf16x8, b), c, 0, 0, 0); }
__device__ __forceinline__ v2u trd(LAS unsigned char* p) { return __builtin_bit_cast(v2u, __builtin_amdgcn_ds_read_tr16_b64_v4i16((LAS s16x4*)p)); }
__device__ __forceinline__ v4u trd2(LAS unsigned char* p0, LAS unsigned char* p1) { v2u a = trd(p0), b = trd(p1); return v4u{a.x, a.y, b.x, b.y}; }
template <int C> __device__ __forceinline__ float dppf(float x) { return __builtin_bit_cast(float, __builtin_amdgcn_mov_dpp(__builtin_bit_cast(int, x), C, 0xf, 0xf, true)); }
template <int C> __device__ __forceinline__ unsigned dppu(unsigned x) { return (unsigned)__builtin_amdgcn_mov_dpp((int)x, C, 0xf, 0xf, true); }
constexpr int QX1 = 0xB1, QX2 = 0x4E, HMIR = 0x141, ROR4 = 0x124, ROR8 = 0x128;
__device__ __forceinline__ float x16_sum(float x) { auto s = __builtin_amdgcn_permlane16_swap(__float_as_uint(x), __float_as_uint(x), false, false); return __uint_as_float(s[0]) + __uint_as_float(s[1]); }
__device__ __forceinline__ float x32_sum(float x) { auto s = __builtin_amdgcn_permlane32_swap(__float_as_uint(x), __float_as_uint(x), false, false); return __uint_as_float(s[0]) + __uint_as_float(s[1]); }
__device__ __forceinline__ float x16_max(float x) { auto s = __builtin_amdgcn_permlane16_swap(__float_as_uint(x), __float_as_uint(x), false, false); return fmaxf(__uint_as_float(s[0]), __uint_as_float(s[1])); }
__device__ __forceinline__ float x32_max(float x) { auto s = __builtin_amdgcn_permlane32_swap(__float_as_uint(x), __float_as_uint(x), false, false); return fmaxf(__uint_as_float(s[0]), __uint_as_float(s[1])); }
__device__ __forceinline__ float sum8(float x) { x += dppf<QX1>(x); x += dppf<QX2>(x); x += dppf<HMIR>(x); return x; }
__device__ __forceinline__ float max8(float x) { x = fmaxf(x, dppf<QX1>(x)); x = fmaxf(x, dppf<QX2>(x)); x = fmaxf(x, dppf<HMIR>(x)); return x; }
__device__ __forceinline__ float sum64(float x) { x = sum8(x); x += dppf<ROR8>(x); return x32_sum(x16_sum(x)); }
#define LGKM0() asm volatile("s_waitcnt lgkmcnt(0)" ::: "memory")
#define VM0() asm volatile("s_waitcnt vmcnt(0)" ::: "memory")
#define VMN(n) asm volatile("s_waitcnt vmcnt(%0)" :: "n"(n) : "memory")
#define BAR() do { asm volatile("s_waitcnt lgkmcnt(0)" ::: "memory"); __builtin_amdgcn_s_barrier(); asm volatile("" ::: "memory"); } while (0)
#define SB() __builtin_amdgcn_sched_barrier(0)
#define PIN(x) asm volatile("" : "+v"(x))
__device__ __forceinline__ void set_flag(LAS unsigned char* L, int f) { LGKM0(); *(volatile LAS unsigned*)(L + L_FLG + 4 * f) = 1u; }
__device__ __forceinline__ void wait_flag(LAS unsigned char* L, int f) {
  while (__builtin_amdgcn_readfirstlane(*(volatile LAS unsigned*)(L + L_FLG + 4 * f)) == 0u) __builtin_amdgcn_s_sleep(1);
  asm volatile("" ::: "memory");
}
__device__ __forceinline__ void wait_flag2(LAS unsigned char* L, int f) {
  for (;;) { v2u v = *(volatile LAS v2u*)(L + L_FLG + 4 * f); if (__builtin_amdgcn_readfirstlane(v.x & v.y)) break; __builtin_amdgcn_s_sleep(1); }
  asm volatile("" ::: "memory");
}
__device__ __forceinline__ void wait_flag4(LAS unsigned char* L, int f) {
  for (;;) { v4u v = *(volatile LAS v4u*)(L + L_FLG + 4 * f); if (__builtin_amdgcn_readfirstlane(v.x & v.y & v.z & v.w)) break; __builtin_amdgcn_s_sleep(1); }
  asm volatile("" ::: "memory");
}
__device__ __forceinline__ void until(unsigned t) { while ((int)((unsigned)__builtin_amdgcn_s_memrealtime() - t) < 0) __builtin_amdgcn_s_sleep(1); }

__device__ __forceinline__ v4u tag4(unsigned e) { return v4u{e & 1023u, (e + (e >> 10)) & 1023u, (e + (e >> 20)) & 1023u, (e + (e >> 10) + (e >> 22)) & 1023u}; }
__device__ __forceinline__ v4u enc4(f32x4 v, v4u t) { return (__builtin_bit_cast(v4u, v) & ~1023u) | t; }

__device__ __forceinline__ unsigned or4(v4u y) { return (y.x | y.y) | (y.z | y.w); }
#define G_EACH(M) M(0) M(1) M(2) M(3) if constexpr (N > 4) { M(4) } if constexpr (N == 8) { M(5) M(6) M(7) }
template <int N, int STR, class MID> __device__ __forceinline__ f32x4 gather(rsrc_t r, int vo, v4u t, int lane, MID mid) {
  v4u y0, y1, y2, y3, y4, y5, y6, y7;
  unsigned m0, m1, m2, m3, m4, m5, m6, m7, a = 0;
#define G_LD(i) y##i = ld128<AUX_SC1>(r, vo, i * STR); SB();
#define G_CHK(i) y##i ^= t; if (N == 5 && i == 4) y##i = lane < 32 ? y##i : v4u{0u, 0u, 0u, 0u}; m##i = or4(y##i); a |= m##i;
#define G_RLD(i) if (m##i & 1023u) y##i = ld128<AUX_SC1>(r, vo, i * STR);
#define G_RCK(i) if (m##i & 1023u) { y##i ^= t; m##i = or4(y##i); a |= m##i; }
  G_EACH(G_LD)
  mid();
  SB();
  f32x4 sum;
  G_CHK(0) SB(); G_CHK(1) f32x4 p01 = asf(y0) + asf(y1); PIN(p01); SB(); G_CHK(2) SB(); G_CHK(3) f32x4 p03 = p01 + (asf(y2) + asf(y3)); PIN(p03); PIN(a); SB();
  if constexpr (N > 4) { G_CHK(4) }
  if constexpr (N == 8) { SB(); G_CHK(5) f32x4 p45 = asf(y4) + asf(y5); PIN(p45); SB(); G_CHK(6) PIN(a); SB(); G_CHK(7) sum = p03 + (p45 + (asf(y6) + asf(y7))); }
  else if constexpr (N == 5) sum = p03 + asf(y4);
  else sum = p03;
  PIN(sum);
  if (__builtin_expect(__builtin_amdgcn_ballot_w64((a & 1023u) != 0u) != 0ull, 0)) {
    for (int s = 0; s < SPIN; ++s) {
      asm volatile("" ::: "memory");
      G_EACH(G_RLD)
      a = 0;
      G_EACH(G_RCK)
      if (__builtin_amdgcn_ballot_w64((a & 1023u) != 0u) == 0ull) break;
    }
    sum = (asf(y0) + asf(y1)) + (asf(y2) + asf(y3));
    if constexpr (N == 8) sum += (asf(y4) + asf(y5)) + (asf(y6) + asf(y7)); else if constexpr (N == 5) sum += asf(y4);
  }
  return sum;
}
template <int N, int STR, int SLP> __device__ __forceinline__ void gatherg(v4u (&v)[N], rsrc_t r, int vo, int vlast, unsigned tag) {
#pragma unroll
  for (int i = 0; i < N; ++i) v[i] = i < N - 1 ? ld128<AUX_SC1>(r, vo, i * STR) : ld128<AUX_SC1>(r, vlast, 0);
  for (int s = 0; s < SPIN; ++s) {
    unsigned m[N], a = 0;
#pragma unroll
    for (int i = 0; i < N; ++i) { m[i] = (v[i].y ^ tag) | (v[i].w ^ tag); a |= m[i]; }
    if (__builtin_amdgcn_ballot_w64(a != 0u) == 0ull) break;
    asm volatile("" ::: "memory");
    __builtin_amdgcn_s_sleep(SLP);
#pragma unroll
    for (int i = 0; i < N; ++i) if (m[i] != 0u) v[i] = i < N - 1 ? ld128<AUX_SC1>(r, vo, i * STR) : ld128<AUX_SC1>(r, vlast, 0);
  }
}
template <int NS> __device__ __forceinline__ f32x4 gemv(const v4u (&a)[18], unsigned pb) {
  v4u bq[NS];
#pragma unroll
  for (int s = 0; s < NS; ++s) bq[s] = *(LAS v4u*)(size_t)(pb + 64 * s);
  SB();
  f32x4 d = {0.f, 0.f, 0.f, 0.f};
#pragma unroll
  for (int s = 0; s < NS; ++s) d = mfma(a[s], bq[s], d);
  return d;
}
template <int NH> __device__ __forceinline__ f32x2 gemv2(const v4u (&a)[18], unsigned pb) {
  v4u bq[NH];
#pragma unroll
  for (int s = 0; s < NH; ++s) bq[s] = *(LAS v4u*)(size_t)(pb + 64 * s);
  SB();
  f32x4 d0 = {0.f, 0.f, 0.f, 0.f}, d1 = {0.f, 0.f, 0.f, 0.f};
#pragma unroll
  for (int s = 0; s < NH; ++s) { d0 = mfma(bq[s], a[s], d0); d1 = mfma(bq[s], a[NH + s], d1); }
  return f32x2{d0[0], d0[1]} + f32x2{d1[2], d1[3]};
}
#define DUE(i) (T1 + (i) * TAU + ((i) > 128 - NSL ? ((i) - (128 - NSL)) * DSL : 0))
constexpr float LOG2E = 1.4426950408889634f;

constexpr int KR = 64 - NL, PRE = RING + NL;
constexpr int slot(int i) { return (i < KR ? i : i - NL) % RING; }
constexpr int issued(int pr) {
  return pr < 0 ? PRE : PRE + (pr < 2 ? 2 * (pr + 1) : 4 + (pr - 1) / 2);
}
constexpr int ISM = issued(31) + 3;
constexpr int issuedv(int u) {
  if (u < 0) return ISM;
  const int a = ISM + (u + 1) / 2, b = 64 + u + 1 + TV, m = a > b ? a : b;
  return m < 128 ? m : 128;
}
constexpr int last_issue() { int u = 0; while (issuedv(u) < 128) ++u; return u; }
constexpr bool one_per_step() { for (int u = 0; u < 64; ++u) if (issuedv(u) - issuedv(u - 1) > 1) return false; return true; }
static_assert(one_per_step() && ISM - 64 <= RING && (PRE >= 64 || NL == 0) && NL % 2 == 0, "ring");

typedef unsigned v16u __attribute__((ext_vector_type(16)));
typedef unsigned v8u __attribute__((ext_vector_type(8)));
__device__ __forceinline__ u16* ptr_of(unsigned lo, unsigned hi) { return (u16*)(((uint64_t)hi << 32) | lo); }

__global__ void __launch_bounds__(NTHR, 2) fused(const u16*, const u16*, const u16*, const u16*, const u16*, const u16*, const u16*, const u16*, const u16*, const u16*,
                                                 const u16*, u16*, unsigned char*) {
  v16u ka;
  v8u kb;
  v2u kd;
  asm volatile("s_load_dwordx16 %0, %3, 0x0\n\ts_load_dwordx8 %1, %3, 0x40\n\ts_load_dwordx2 %2, %3, 0x60"
               : "=&s"(ka), "=&s"(kb), "=&s"(kd) : "s"(__builtin_amdgcn_kernarg_segment_ptr()));
  extern __shared__ __attribute__((aligned(16))) unsigned char smem[];
  LAS unsigned char* L = (LAS unsigned char*)smem;
  const int wg = blockIdx.x, tid = threadIdx.x, lane = tid & 63, w = __builtin_amdgcn_readfirstlane(tid >> 6);
  const int xl = wg & 7, il = wg >> 3;
  const int n16 = lane & 15, g4 = lane >> 4;
  const bool is_wo = il < 9;
  const bool is_mlp = il >= 9 && il < 18;
  const bool is_q = il >= 18 && il < 26;
  const int mr = 9 * xl + il - 9, qs = 8 * xl + il - 18, ms = (il - 26) * 8 + xl;
  const bool is_mod = il >= 26 && ms < 45;
  v4u cpf0, cpf1;
  if (w == 1) {
    const rsrc_t rc = mkrs((const void*)(__builtin_amdgcn_s_getpc() & ~255ull), CODEPF);
    cpf0 = ld128<0>(rc, il * 1024 + lane * 16, 0);
    cpf1 = ld128<0>(rc, il * 1024 + lane * 16, 32768);
  }
  const unsigned long long tstart64 = __builtin_amdgcn_s_memrealtime();
  const unsigned tstart = (unsigned)tstart64;
  const int ab = (w >> 1) & 1;
  int rs_ = is_wo ? 1152 : is_mlp ? 9216 : is_q ? 8192 : 6912;
  int vm = (is_wo ? 512 * xl + 8 * ((lane >> 3) & 3) + (lane >> 5) + 2 * ab : 8 * ab + (lane >> 3)) * rs_
           + (is_wo ? il : is_mlp ? mr : is_q ? qs : ms) * 128 + ((lane & 7) ^ ((((lane >> 4) & 1) | (ab << 1)) << 1)) * 16;
  int nblk = is_wo ? 32 : (il < 26 || is_mod) ? 36 : 0;
  PIN(vm);
  asm volatile("" : "+s"(rs_), "+s"(nblk));
  if (w == 0) {
    const v4u s4 = {SENT, SENT, SENT, SENT};
#pragma unroll
    for (int i = 0; i < 4; ++i) *(LAS v4u*)(L + L_MRG + i * 1024 + lane * 16) = s4;
    *(LAS unsigned*)(L + L_FLG + lane * 4) = 0u;
  }
  asm volatile("s_waitcnt lgkmcnt(0)" : "+s"(ka), "+s"(kb), "+s"(kd) : "s"(tstart64) : "memory");
  const u16 *x = ptr_of(ka[0], ka[1]), *kc = ptr_of(ka[2], ka[3]), *vc = ptr_of(ka[4], ka[5]), *wq = ptr_of(ka[6], ka[7]), *wo = ptr_of(ka[8], ka[9]), *w1 = ptr_of(ka[10], ka[11]);
  const u16 *b1 = ptr_of(ka[12], ka[13]), *w2 = ptr_of(ka[14], ka[15]), *b2 = ptr_of(kb[0], kb[1]), *cc = ptr_of(kb[2], kb[3]), *wm = ptr_of(kb[4], kb[5]);
  u16* out = ptr_of(kb[6], kb[7]);
  unsigned char* ws = (unsigned char*)ptr_of(kd[0], kd[1]);
  BAR();
  const int kvh = 2 * xl + (il >> 4), bb = (il >> 3) & 1, sp = il & 7, pair = bb * HKV + kvh;
  const rsrc_t rm = mkrs(is_wo ? wo : is_mlp ? w1 : is_q ? wq : wm, (is_wo ? NQ : D) * rs_);
  unsigned probe = 0;
  if (w < 4) probe = ld32<AUX_NT>(rm, (is_wo ? 512 * xl : 0) * rs_ + (is_wo ? il : is_mlp ? mr : is_q ? qs : ms) * 128, 0);
  if ((w & 5) == 4) {
    __builtin_amdgcn_s_setprio(3);
#pragma clang loop unroll(disable)
    for (int i = 0; i < nblk; ++i) {
      const int r = is_wo ? 4 * (i & 1) + 64 * (i & 2) + 8 * (i & 12) + 16 * (i & 16) : 16 * i;
      VMN(BW - 1);
      dma(rm, L + L_WT + ab * 1024 + i * 2048, vm, r * rs_);
    }
    asm volatile("ds_write_b32 %0, %1" :: "v"((unsigned)(size_t)(L + L_FLG + 4 * (F_I0 + ab))), "v"(1u) : "memory");
  }
  const rsrc_t rw = mkrs(ws, WS_END);
#define LOAD_TAG() ({ const unsigned char* p_ = ws; asm volatile("" : "+s"(p_)); *(const __attribute__((address_space(4))) unsigned*)(uintptr_t)(p_ + WS_EPOCH) + 1u; })
#define UNTIL_ALL(T) do { until(tstart + (T) / 10); until(tstart + (T) / 10 + __builtin_amdgcn_readfirstlane(*(volatile LAS unsigned*)(L + L_FLG + 4 * F_TMX))); } while (0)

  if (w < 4) {
    asm volatile("" :: "v"(probe));
    const unsigned tpr = (unsigned)__builtin_amdgcn_s_memrealtime() - NOM1 / 10;
    unsigned tag = LOAD_TAG();
    asm volatile("" : "+s"(tag));
    if (w == 1) asm volatile("" :: "v"(cpf0), "v"(cpf1));
    unsigned tz = tpr;
    if (w == 0 && il >= 8 && il < 10) st128(rw, lane == 0 ? WS_T0 + (wg & 15) * 16 : WS_END, v4u{tz, tag, tz, tag});
    v4u tu;
    bool tknown = false;
#define T0_ASK() do { if (!tknown) tu = ld128<AUX_SC1>(rw, WS_T0 + n16 * 16, 0); } while (0)
#define T0_USE() do { if (!tknown && __builtin_amdgcn_ballot_w64(((tu.y ^ tag) | (tu.w ^ tag)) != 0u) == 0ull) { \
      tknown = true; \
      const float d_ = fminf(fmaxf((float)(int)(tu.x - tstart), -500.f), 500.f); \
      const float e_ = fminf(d_, dppf<ROR8>(d_));        \
      const float mx_ = max8(e_); \
      float h_ = e_ + dppf<QX1>(e_); \
      h_ += dppf<QX2>(h_);         \
      const float g_ = dppf<HMIR>(h_); \
      int o_ = (int)((h_ + g_) * (0.125f * (1.f - BETA - BETH)) + mx_ * BETA + fmaxf(h_, g_) * (0.25f * BETH)); \
      PIN(o_); \
      tz = tstart + __builtin_amdgcn_readfirstlane(o_); \
      if (w == 0) *(volatile LAS unsigned*)(L + L_FLG + 4 * F_TMX) = (unsigned)(int)fmaxf(mx_, 0.f); \
    } } while (0)
    const rsrc_t rk = mkrs(kc + ((size_t)pair * SK + sp * 1024) * DH, 262144), rv = mkrs(vc + ((size_t)pair * SK + sp * 1024) * DH, 262144);
    const v4u rk4 = mkrs4(kc + ((size_t)pair * SK + sp * 1024) * DH, 262144);
    const int vk = w * 1024 + (n16 >> 2) * 256 + g4 * 64 + (n16 & 3) * 16, vv = w * 1024 + lane * 16;
    v4u ring[RING];
    int so = 0;
#define ITEM(i) ({ if ((i) == 64) so = 0; asm volatile("" : "+s"(so)); const v4u r_ = (i) < 64 ? ld128<AUX_NT>(rk, vk, so) : ld128<AUX_NT>(rv, vv, so); so += 4096; r_; })
#define FLAG(f) (*(volatile LAS unsigned*)(L + L_FLG + 4 * (f)))
    bool sh0 = false, sh1 = false;
#define HOOK(i) do { \
      if ((i) == ICHK + 23 && w == 0) FLAG(F_TZ) = tz | 1u; \
      if (((i) == CK0 || (i) == CK1) && w == 0) FLAG((i) == CK0 ? F_LT0 : F_LT1) = (unsigned)__builtin_amdgcn_s_memrealtime() - tz - DUE(i) / 10 + 1000u; \
      if (((i) == CK0 + CKD || (i) == CK0 + CKD + 8) && !sh0) { const unsigned v_ = __builtin_amdgcn_readfirstlane(FLAG(F_SH0)); sh0 = v_ != 0u; tz += v_ >> 1; } \
      if (((i) == CK1 + CKD || (i) == CK1 + CKD + 6) && !sh1) { const unsigned v_ = __builtin_amdgcn_readfirstlane(FLAG(F_SH1)); sh1 = v_ != 0u; tz += v_ >> 1; } \
    } while (0)
#define PACE(i) do { if ((i) % 2 == 0) asm volatile("1:\n\ts_memrealtime vcc\n\ts_waitcnt lgkmcnt(0)\n\ts_sub_i32 vcc_lo, vcc_lo, %0\n\ts_cmpk_lt_i32 vcc_lo, %1\n\t" \
                       "s_cbranch_scc1 1b\n\ts_cmpk_gt_i32 vcc_lo, %3\n\ts_cbranch_scc1 3f\n\ts_cmpk_gt_i32 vcc_lo, %2\n\ts_cbranch_scc1 2f\n\ts_waitcnt vmcnt(%4)\n2:\n\ts_waitcnt vmcnt(%5)\n3:\n\ts_waitcnt vmcnt(%6)" \
                       :: "s"(tz), "n"(DUE(i) / 10), "n"((DUE(i) + LATE) / 10), "n"((DUE(i) + LATE2) / 10), "n"(THL - 2), "n"(THL - 1), "n"(TH - 2) : "vcc", "scc"); } while (0)
    if (w == 2) {
      const v4u s4 = {SENT, SENT, SENT, SENT}, z4 = {0u, 0u, 0u, 0u};
#pragma unroll
      for (int i = 0; i < 4; ++i) *(LAS v4u*)(L + L_X1 + i * 1024 + lane * 16) = s4;
      if (lane < 48) *(LAS v4u*)(L + L_X1 + 4096 + lane * 16) = lane < 32 ? s4 : z4;
    }
    const unsigned lk = (unsigned)(size_t)(L + L_PK) + w * (NL * 1024);
    wait_flag2(L, F_I0);
#pragma unroll
    for (int i = 0; i < PRE; ++i) {
      PACE(i);
      if (i >= KR && i < 64) {
        unsigned keep;
        asm volatile("s_mov_b32 %0, m0\n\ts_mov_b32 m0, %4\n\ts_nop 0\n\tbuffer_load_dwordx4 %1, %2, %3 offen nt lds\n\ts_mov_b32 m0, %0"
                     : "=&s"(keep) : "v"(vk), "s"(rk4), "s"(so), "s"(lk + (i - KR) * 1024) : "memory");
        so += 4096;
      } else ring[slot(i)] = ITEM(i);
      if (i == ICHK + 10 || i == ICHK + 22) T0_USE();
      if (i == ICHK || i == ICHK + 12) T0_ASK();
      HOOK(i);
      SB();
    }
    wait_flag(L, F_Q);
    const v4u qf = *(LAS v4u*)(L + L_Q + (n16 >> 3) * 256 + (4 * g4 + (n16 & 3)) * 16);
    const v4u z4 = {0u, 0u, 0u, 0u};
    const v4u q0 = (lane & 4) ? z4 : qf, q1 = (lane & 4) ? qf : z4;
    float sc[8];
#pragma unroll
    for (int pr = 0; pr < 32; ++pr) {
      f32x4 d = {0.f, 0.f, 0.f, 0.f};
      if (2 * pr < KR) {
        d = mfma(ring[slot(2 * pr)], q0, d);
        d = mfma(ring[slot(2 * pr + 1)], q1, d);
      } else {
        if (2 * pr == KR) VMN(issued(KR / 2 - 1) - 64 < 63 ? issued(KR / 2 - 1) - 64 : 63);
        LAS unsigned char* pk_ = L + L_PK + w * (NL * 1024) + (2 * pr - KR) * 1024 + lane * 16;
        d = mfma(*(LAS v4u*)pk_, q0, d);
        d = mfma(*(LAS v4u*)(pk_ + 1024), q1, d);
      }
      SB();
      if (issued(pr - 1) < issued(pr)) { const int i = issued(pr - 1); PACE(i); ring[slot(i)] = ITEM(i); HOOK(i); }
      if (issued(pr - 1) + 1 < issued(pr)) { const int i = issued(pr - 1) + 1; PACE(i); ring[slot(i)] = ITEM(i); HOOK(i); }
      const float s = (dppf<0x00>(d[0]) + dppf<0x55>(d[1])) + (dppf<0xAA>(d[2]) + dppf<0xFF>(d[3]));
      sc[pr >> 2] = (pr & 3) == 0 ? s : ((lane & 3) == (pr & 3) ? s : sc[pr >> 2]);
      PIN(sc[pr >> 2]);
      SB();
    }
#define ISSUE1(i) do { PIN(m); SB(); PACE(i); ring[slot(i)] = ITEM(i); HOOK(i); SB(); } while (0)
    float m = -INFINITY;
#pragma unroll
    for (int a = 0; a < 8; ++a) { sc[a] = rb(rb(sc[a]) * 0.08838834764831845f); m = fmaxf(m, sc[a]); }
    ISSUE1(ISM - 3);
    m = x32_max(x16_max(max8(m)));
    ISSUE1(ISM - 2);
    float l = 0.f;
    unsigned pp[8], pm[8];
#pragma unroll
    for (int a = 0; a < 8; ++a) { const float p = __builtin_amdgcn_exp2f((sc[a] - m) * LOG2E); l += p; pp[a] = pk(p, p); pm[a] = dppu<HMIR>(pp[a]); }
    PIN(l);
    ISSUE1(ISM - 1);
    l = x32_sum(x16_sum(sum8(l)));
    if ((lane & 55) == 0) *(LAS f32x2*)(L + L_WML + w * 16 + (lane >> 3) * 8) = f32x2{m, l};
    set_flag(L, F_ML0 + w);
    const unsigned hm = (lane & 1) ? 0xffff0000u : 0x0000ffffu;
    const int ci = (lane & 7) >> 1;
    const unsigned mk0 = ci == 0 ? hm : 0u, mk1 = ci == 1 ? hm : 0u, mk2 = ci == 2 ? hm : 0u, mk3 = ci == 3 ? hm : 0u;
    f32x4 acc = {0.f, 0.f, 0.f, 0.f};
    unsigned tl = 0;
    SB();
#pragma unroll
    for (int u = 0; u < 64; ++u) {
      const int a = u >> 3, v0 = (u >> 1) & 3, t0 = u & 1;
      unsigned own, oth;
      switch (v0) {
        case 0: own = dppu<0x00>(pp[a]); oth = dppu<0xFF>(pm[a]); break;
        case 1: own = dppu<0x55>(pp[a]); oth = dppu<0xAA>(pm[a]); break;
        case 2: own = dppu<0xAA>(pp[a]); oth = dppu<0x55>(pm[a]); break;
        default: own = dppu<0xFF>(pp[a]); oth = dppu<0x00>(pm[a]); break;
      }
      const unsigned lo = t0 == 0 ? own : oth, hi = t0 == 0 ? oth : own;
      const v4u bp = {lo & mk0, lo & mk1, hi & mk2, hi & mk3};
      acc = mfma(ring[slot(64 + u)], bp, acc);
      SB();
      if (issuedv(u - 1) < issuedv(u)) { const int i = issuedv(u - 1); PACE(i); ring[slot(i)] = ITEM(i); HOOK(i); }
      if (u == last_issue()) { tl = (unsigned)__builtin_amdgcn_s_memrealtime() | 1u; *(volatile LAS unsigned*)(L + L_FLG + 4 * (F_V0 + w)) = tl; }
      SB();
    }
    *(LAS v4u*)(L + L_MRG + w * 1024 + lane * 16) = nos4(acc);
    const v4u t4 = tag4(tag);
    if (is_wo) {
      unsigned po = (unsigned)(size_t)(L + L_O) + w * 576 + lane * 8;
      PIN(po);
      until(tl + TP1 / 10);
      const f32x4 o = gather<8, 1024>(rw, WS_H1 + ((w >> 1) * HKV + 2 * xl + (w & 1)) * 8192 + lane * 16, t4, lane, [] {});
      *(LAS v2u*)(size_t)po = v2u{pk(o[0], o[1]), pk(o[2], o[3])};
      BAR();
    } else if (is_mlp) {
      wait_flag(L, F_RDY);
      wait_flag(L, F_ML0 + 2);
      const int b_ = w >> 1, hf = w & 1;
      f32x2 c1[3], sbv[3];
#pragma unroll
      for (int i = 0; i < 3; ++i) {
        const int k = lane + 64 * (i < 2 ? 2 * hf + i : 4);
        c1[i] = rb2(unpk(*(LAS unsigned*)(L + L_MOD + b_ * 3584 + 2304 + k * 4)) + 1.f);
        sbv[i] = unpk(*(LAS unsigned*)(L + L_MOD + b_ * 3584 + 1152 + k * 4));
        PIN(c1[i]); PIN(sbv[i]);
      }
      unsigned pr_ = (unsigned)(size_t)(L + L_X1) + b_ * 2304 + lane * 8, pw_ = (unsigned)(size_t)(L + L_H2) + b_ * 1408 + lane * 4;
      unsigned pw2 = pw_ + 512 * hf;
      PIN(pr_); PIN(pw_); PIN(pw2);
      LAS unsigned char* p2 = L + L_WT + (8 * g4 + (n16 >> 2)) * 1152 + (16 + hf) * 64 + 8 * (lane & 3);
      unsigned ph = (unsigned)(size_t)(L + L_H1) + (lane & 1) * 128 + g4 * 16;
      int v3 = WS_H3 + mr * 4608 + (16 + hf) * 256 + lane * 16;
      v4u tv = t4;
      PIN(ph); PIN(v3); PIN(tv);
      {
        f32x2 x0 = unpk(*(LAS unsigned*)(L + L_X + tid * 4)), x1_ = unpk(*(LAS unsigned*)(L + L_X + 1152 + tid * 4));
        f32x2 g0 = unpk(*(LAS unsigned*)(L + L_MOD + tid * 4)), g1 = unpk(*(LAS unsigned*)(L + L_MOD + 3584 + tid * 4));
        unsigned px = (unsigned)(size_t)(L + L_X1) + tid * 8;
        PIN(x0); PIN(x1_); PIN(g0); PIN(g1); PIN(px);
        until(tl + TP2 / 10);
        const f32x4 s = gather<8, 4608>(rw, WS_H2 + tid * 16, t4, lane, [] {});
        *(LAS f32x2*)(size_t)px = rb2(x0 + rb2(g0 * rb2(f32x2{s[0], s[2]})));
        *(LAS f32x2*)(size_t)(px + 2304) = rb2(x1_ + rb2(g1 * rb2(f32x2{s[1], s[3]})));
      }
      f32x2 xv[5];
      for (;;) {
        v2u u0 = *(volatile LAS v2u*)(size_t)pr_, u1 = *(volatile LAS v2u*)(size_t)(pr_ + 512), u2 = *(volatile LAS v2u*)(size_t)(pr_ + 1024);
        v2u u3 = *(volatile LAS v2u*)(size_t)(pr_ + 1536), u4 = *(volatile LAS v2u*)(size_t)(pr_ + 2048);
        xv[0] = __builtin_bit_cast(f32x2, u0); xv[1] = __builtin_bit_cast(f32x2, u1); xv[2] = __builtin_bit_cast(f32x2, u2); xv[3] = __builtin_bit_cast(f32x2, u3); xv[4] = __builtin_bit_cast(f32x2, u4);
        unsigned o_ = (u0.x | u0.y) | u1.x;
        PIN(o_); o_ = (o_ | u1.y) | u2.x; PIN(o_); o_ = (o_ | u2.y) | u3.x; PIN(o_); o_ = (o_ | u3.y) | u4.x; PIN(o_);
        if (__builtin_amdgcn_ballot_w64(((o_ | u4.y) & 1u) != 0u) == 0ull) break;
      }
      const float pv_ = __builtin_bit_cast(float, __builtin_amdgcn_readfirstlane(__builtin_bit_cast(int, xv[0].x)));
      f32x2 dv[5];
#pragma unroll
      for (int i = 0; i < 5; ++i) dv[i] = xv[i] - pv_;
      dv[4] = lane < 32 ? dv[4] : f32x2{0.f, 0.f};
      const f32x2 a1 = ((dv[0] + dv[1]) + (dv[2] + dv[3])) + dv[4];
      const f32x2 a2_ = (dv[0] * dv[0] + dv[1] * dv[1]) + (dv[2] * dv[2] + dv[3] * dv[3] + dv[4] * dv[4]);
      const auto sw_ = __builtin_amdgcn_permlane32_swap(__float_as_uint(a1.x + a1.y), __float_as_uint(a2_.x + a2_.y), false, false);
      float r_ = sum8(__uint_as_float(sw_[0]) + __uint_as_float(sw_[1]));
      r_ += dppf<ROR8>(r_);
      r_ = x16_sum(r_) * (1.f / D);
      const auto s12 = __builtin_amdgcn_permlane32_swap(__float_as_uint(r_), __float_as_uint(r_), false, false);
      const float s1 = __uint_as_float(s12[0]), s2 = __uint_as_float(s12[1]);
      const f32x2 mv_ = rb2(f32x2{pv_ + s1, fmaxf(s2 - s1 * s1, 0.f)});
      const float rden = __builtin_amdgcn_rcpf(rb(__builtin_amdgcn_sqrtf(rb(mv_.y + 1e-5f))));
      f32x2 h[3];
#define STAGE(E) do { _Pragma("unroll") for (int i = 0; i < 3; ++i) h[i] = E; asm volatile("" : "+v"(h[0]), "+v"(h[1]), "+v"(h[2])); } while (0)
      STAGE(rb2((i == 2 ? xv[4] : hf ? xv[2 + i] : xv[i]) - mv_.x));
      STAGE(rb2(h[i] * rden));
      STAGE(rb2(h[i] * c1[i]));
      STAGE(h[i] + sbv[i]);
#pragma unroll
      for (int i = 0; i < 3; ++i) *(LAS unsigned*)(size_t)(i == 2 ? pw_ + 1024 : pw2 + 256 * i) = pk(h[i].x, h[i].y);
      BAR();
      if (w < 2) {
        __builtin_amdgcn_s_sleep(2);
        const v4u a0 = trd2(p2, p2 + 4 * 1152), a1_ = trd2(p2 + 32 * 1152, p2 + 36 * 1152), a2 = trd2(p2 + 32, p2 + 32 + 4 * 1152), a3 = trd2(p2 + 32 + 32 * 1152, p2 + 32 + 36 * 1152);
        BAR();
        const v4u bq0 = *(LAS v4u*)(size_t)ph, bq1 = *(LAS v4u*)(size_t)(ph + 64);
        SB();
        const f32x4 z_ = {0.f, 0.f, 0.f, 0.f};
        const f32x4 e0 = mfma(bq1, a1_, mfma(bq0, a0, z_)), e1 = mfma(bq1, a3, mfma(bq0, a2, z_));
        if (lane < 16) st128(rw, v3, enc4(f32x4{e0[0], e0[1], e1[0], e1[1]}, tv));
      } else BAR();
    }
  } else {
    const int hw = w - 4, lq = lane < 48 ? lane : 47;
    float xv[12];
    unsigned tag;
    if (!(w & 1)) {
      tag = LOAD_TAG();
      VM0();
      asm volatile("" : "+s"(tag));
      set_flag(L, F_D0 + ab);
    } else {
      __builtin_amdgcn_s_setprio(3);
#define STOUCH(P) do { const u16* p_ = (P); asm volatile("" : "+s"(p_)); const unsigned t_ = *(const __attribute__((address_space(4))) unsigned*)(uintptr_t)p_; asm volatile("" :: "s"(t_)); } while (0)
      if (il < 2) { STOUCH(kc + (size_t)(il * HKV + 2 * xl + ab) * SK * DH); }
      if (il >= 2 && il < 6) { STOUCH((il == 2 ? wq : il == 3 ? w1 : il == 4 ? wm : wo + (size_t)(xl * 512 + 511) * D) + (il == 5 ? 0 : il == 4 ? 1048576 : (ab + 1) * 1048576)); }
      v2u xr[3];
      const rsrc_t rx = mkrs(is_mod ? cc : x, 2304);
#pragma unroll
      for (int i = 0; i < 3; ++i) xr[i] = ld64<0>(rx, ab * 1152 + lq * 8, i * 384);
      tag = LOAD_TAG();
#pragma unroll
      for (int i = 0; i < 3; ++i) { xv[4 * i] = bf_lo(xr[i].x); xv[4 * i + 1] = bf_hi(xr[i].x); xv[4 * i + 2] = bf_lo(xr[i].y); xv[4 * i + 3] = bf_hi(xr[i].y); }
      if (is_mod) {
#pragma unroll
        for (int i = 0; i < 12; ++i) xv[i] = xv[i] / (1.f + expf(-xv[i]));
#pragma unroll
        for (int i = 0; i < 3; ++i) *(LAS v2u*)(L + L_ACT + ab * 1152 + i * 384 + lq * 8) = v2u{pk(xv[4 * i], xv[4 * i + 1]), pk(xv[4 * i + 2], xv[4 * i + 3])};
      } else if (is_q) {
        float s = 0.f;
#pragma unroll
        for (int i = 0; i < 12; ++i) s += xv[i];
        const float mean = sum64(lane < 48 ? s : 0.f) * (1.f / D);
        float q = 0.f;
#pragma unroll
        for (int i = 0; i < 12; ++i) { const float a = xv[i] - mean; q += a * a; }
        const float var = sum64(lane < 48 ? q : 0.f) * (1.f / D);
        const float den = rb(__builtin_sqrtf(rb(rb(var) + 1e-5f))), mb = rb(mean);
#pragma unroll
        for (int i = 0; i < 12; ++i) { xv[i] = rb(rb(xv[i] - mb) / den); PIN(xv[i]); }
      } else {
#pragma unroll
        for (int i = 0; i < 3; ++i) *(LAS v2u*)(L + L_X + ab * 1152 + i * 384 + lq * 8) = xr[i];
      }
      asm volatile("" : "+s"(tag));
      if (!is_q) set_flag(L, F_ACT0 + ab);
    }
    v4u a[18];
    LAS unsigned char* pa = L + L_WT + (8 * g4 + (n16 >> 2)) * 128 + ((hw ^ ((n16 >> 3) | ((g4 & 1) << 1))) * 32) + 8 * (lane & 3);
    wait_flag2(L, F_D0);
#pragma unroll
    for (int s = 0; s < 18; ++s) a[s] = trd2(pa + 32 * s * 128, pa + (32 * s + 4) * 128);
    set_flag(L, F_S0 + hw);
    const unsigned pact = (unsigned)(size_t)(L + L_ACT) + (lane & 1) * 1152 + 16 * g4;
    if (is_q || is_mod) {
      if (is_q && (w & 1)) {
        v4u sg[6];
        UNTIL_ALL(TSG);
        gatherg<6, 768, 8>(sg, rw, WS_MG + (ab * 864 + lq) * 16, WS_MG + (ab * 864 + lq) * 16 + 5 * 768, tag);
#pragma unroll
        for (int i = 0; i < 3; ++i) {
          const f32x2 h01 = rb2(f32x2{xv[4 * i], xv[4 * i + 1]} * rb2(unpk(sg[3 + i].x) + 1.f)) + unpk(sg[i].x);
          const f32x2 h23 = rb2(f32x2{xv[4 * i + 2], xv[4 * i + 3]} * rb2(unpk(sg[3 + i].z) + 1.f)) + unpk(sg[i].z);
          *(LAS v2u*)(L + L_ACT + ab * 1152 + i * 384 + lq * 8) = v2u{pk(h01.x, h01.y), pk(h23.x, h23.y)};
        }
        set_flag(L, F_ACT0 + ab);
      }
      wait_flag2(L, F_ACT0);
      const f32x4 d = gemv<18>(a, pact);
      const int off = is_q ? WS_QG + (n16 * 1024 + qs * 16 + hw * 4 + g4) * 16 : WS_MG + (n16 * 864 + ms * 16 + hw * 4 + g4) * 16;
      st128(rw, n16 < 2 ? off : WS_END, v4u{pk(d[0], d[1]), tag, pk(d[2], d[3]), tag});
    }
    const v4u t4 = tag4(tag);
    const bool is_gb = is_mod && ms < 9;
    if ((w & 1) && il < 4) {
      until(tstart + 650);
      STOUCH(il < 2 ? vc + (size_t)(il * HKV + 2 * xl + ab) * SK * DH : il == 2 ? w2 + (size_t)(xl + ab) * 576 * D - 2 * ab : ab ? out : b2);
    }
    if (w == 7 || (w == 5 && (il >> 1) == 2)) {
      wait_flag(L, F_TZ);
      unsigned tzh = __builtin_amdgcn_readfirstlane(FLAG(F_TZ));
#pragma clang loop unroll(disable)
      for (int c = 0; c < 2; ++c) {
        const unsigned dl = tzh + (c ? DUE(CK1) + CAPL : DUE(CK0) + CAPL) / 10;
        if (w == 5) {
          unsigned v_;
          for (;;) {
            v_ = __builtin_amdgcn_readfirstlane(FLAG(F_LT0 + c));
            if (v_ || (int)((unsigned)__builtin_amdgcn_s_memrealtime() - dl) >= 0) break;
            __builtin_amdgcn_s_sleep(2);
          }
          v_ = v_ ? min(max(v_, 1000u) - 1000u, (unsigned)CAPL / 10) : CAPL / 10;
          st128(rw, lane == 0 ? WS_LT + c * 256 + (wg & 15) * 16 : WS_END, v4u{v_, tag, v_, tag});
          if (c == 0) wait_flag(L, F_SH0);
        } else {
          until(dl + 50);
          v4u u_[1];
          gatherg<1, 0, 16>(u_, rw, 0, WS_LT + c * 256 + n16 * 16, tag);
          const float l_ = (float)min(u_[0].x, (unsigned)CAPL / 10);
          float m_ = sum8(l_ + KX * fmaxf(l_ - LX / 10, 0.f));
          m_ += dppf<ROR8>(m_);
          FLAG(F_SH0 + c) = 2u * (unsigned)fmaxf(m_ * (1.f / 16.f) - DEAD / 10, 0.f) + 1u;
        }
        tzh += (unsigned)__builtin_amdgcn_readfirstlane(FLAG(F_SH0)) >> 1;
      }
    }
    if (w == 6) {
      v4u qg[1];
      UNTIL_ALL(TQP);
      gatherg<1, 0, 8>(qg, rw, 0, WS_QG + (bb * 1024 + kvh * 64 + lane) * 16, tag);
      *(LAS v2u*)(L + L_Q + lane * 8) = v2u{qg[0].x, qg[0].z};
      wait_flag2(L, F_ACT0);
      set_flag(L, F_Q);
      if (is_mlp) {
        const unsigned bv = lane < 32 ? ld32<0>(mkrs(b1, F * 2), mr * 128 + lane * 4, 0) : ld32<0>(mkrs(b2, D * 2), (((mr >> 2) * 2 + ((lane >> 1) & 1)) * 16 + (mr & 3) * 4) * 2 + (lane & 1) * 4, 0);
        if (lane < 36) *(LAS unsigned*)(L + L_BI + lane * 4) = bv;
#pragma unroll
        for (int b_ = 0; b_ < 2; ++b_) {
          v4u mv[7];
          gatherg<7, 1024, 32>(mv, rw, WS_MG + (b_ * 864 + 288 + lane) * 16, WS_MG + (b_ * 864 + 288 + 384 + lq) * 16, tag);
#pragma unroll
          for (int i = 0; i < 7; ++i) *(LAS v2u*)(L + L_MOD + b_ * 3584 + i * 512 + lane * 8) = v2u{mv[i].x, mv[i].z};
        }
        set_flag(L, F_RDY);
      }
    } else if (w == 4) {
      wait_flag4(L, F_ML0);
      const int hd = (lane >> 3) & 1;
      f32x2 ml[4];
#pragma unroll
      for (int i = 0; i < 4; ++i) ml[i] = *(LAS f32x2*)(L + L_WML + i * 16 + hd * 8);
      const float mc = fmaxf(fmaxf(ml[0].x, ml[1].x), fmaxf(ml[2].x, ml[3].x));
      float lc = 0.f;
#pragma unroll
      for (int i = 0; i < 4; ++i) lc += ml[i].y * __builtin_amdgcn_exp2f((ml[i].x - mc) * LOG2E);
      st128(rw, (lane & 55) == 0 ? WS_ML + (pair * 8 + sp) * 32 + hd * 16 : WS_END, v4u{__float_as_uint(mc), tag, __float_as_uint(lc), tag});
      v4u r[1];
      __builtin_amdgcn_s_sleep(48);
      gatherg<1, 0, 24>(r, rw, 0, WS_ML + (pair * 8 + (lane & 7)) * 32 + hd * 16, tag);
      const float ms_ = __uint_as_float(r[0].x), M = max8(ms_);
      const float Lt = sum8(__uint_as_float(r[0].z) * __builtin_amdgcn_exp2f((ms_ - M) * LOG2E));
      const float inv = 1.f / Lt;
      float fw[4];
#pragma unroll
      for (int i = 0; i < 4; ++i) { fw[i] = __builtin_amdgcn_exp2f((ml[i].x - M) * LOG2E) * inv; PIN(fw[i]); }
      int vo = WS_H1 + (pair * 8 + sp) * 1024 + lane * 16;
      v4u tv = t4;
      PIN(vo); PIN(tv);
      wait_flag4(L, F_V0);
      v4u pv[4];
      for (;;) {
#pragma unroll
        for (int i = 0; i < 4; ++i) pv[i] = *(volatile LAS v4u*)(L + L_MRG + i * 1024 + lane * 16);
        if (__builtin_amdgcn_ballot_w64(max(max(mx4(pv[0]), mx4(pv[1])), max(mx4(pv[2]), mx4(pv[3]))) == SENT) == 0ull) break;
      }
      st128(rw, vo, enc4((asf(pv[0]) * fw[0] + asf(pv[1]) * fw[1]) + (asf(pv[2]) * fw[2] + asf(pv[3]) * fw[3]), tv));
      set_flag(L, F_H1);
    } else if (is_mlp || is_gb) {
      const rsrc_t r2 = is_mlp ? mkrs(w2, D * F * 2) : mkrs(wm, D * 6912);
      const int v2 = is_mlp ? mr * 73728 + ab * 1024 + lane * 16 : vm + 45 * 128, st2 = is_mlp ? 2048 : 16 * 6912;
      wait_flag4(L, F_S0);
      wait_flag4(L, F_V0);
      if (is_mlp) until((unsigned)__builtin_amdgcn_s_memrealtime() + TWM / 10);
      else wait_flag(L, F_H1);
#pragma clang loop unroll(disable)
      for (int i = 0; i < 36; ++i) dma(r2, L + L_WT + ab * 1024 + i * 2048, v2, i * st2);
      VM0();
      set_flag(L, F_W0 + ab);
    }
    if (is_wo) {
      unsigned pb = (unsigned)(size_t)(L + L_O) + (lane & 1) * 1152 + ((lane >> 1) & 1) * 576 + g4 * 16;
      int vo = lane < 16 ? WS_H2 + xl * 4608 + (il * 64 + hw * 16 + lane) * 8 : WS_END;
      unsigned ta = (lane & 1) ? t4.z : t4.x, tb = (lane & 1) ? t4.w : t4.y;
      PIN(pb); PIN(vo); PIN(ta); PIN(tb);
      BAR();
      const f32x2 d = gemv2<8>(a, pb);
      __builtin_amdgcn_raw_buffer_store_b64(v2u{(__float_as_uint(d[0]) & ~1023u) | ta, (__float_as_uint(d[1]) & ~1023u) | tb}, rw, vo, 0, AUX_SC1);
    } else if (is_mlp) {
      wait_flag(L, F_RDY);
      const unsigned tl = __builtin_amdgcn_readfirstlane(*(LAS unsigned*)(L + L_FLG + 4 * F_V0));
      unsigned ph = (unsigned)(size_t)(L + L_H1) + (lane & 1) * 128 + g4 * 16, mkv = ~1023u;
      int v3 = WS_H3 + mr * 4608 + hw * 1024 + lane * 16;
      v4u tv = t4;
      PIN(v3); PIN(ph); PIN(tv); PIN(mkv);
      const v4u rw4 = mkrs4(ws, WS_END);
      v4u a2[16];
      LAS unsigned char* p2 = L + L_WT + (8 * g4 + (n16 >> 2)) * 1152 + hw * 256 + 8 * (lane & 3);
      float b1v = bf_lo(*(LAS u16*)(L + L_BI + (hw * 16 + n16) * 2));
      unsigned pb = (unsigned)(size_t)(L + L_H2) + (lane & 1) * 1408 + ((lane >> 1) & 1) * 576 + 16 * g4;
      unsigned pw_ = (unsigned)(size_t)(L + L_H1) + (hw * 16 + n16) * 2;
      PIN(b1v); PIN(pb); PIN(pw_);
      __builtin_amdgcn_s_setprio(0);
      const int oj = (lane >> 2) & 3, oc = ((mr >> 2) * 2 + (oj >> 1)) * 16 + (mr & 3) * 4 + (lane & 3);
      const int vg = w == 5 ? WS_MG + ((oj & 1) * 864 + 720 + (oc >> 2)) * 16 : WS_END;
      float b2v = bf_lo(*(LAS u16*)(L + L_BI + 128 + ((oj >> 1) * 4 + (lane & 3)) * 2));
      int vout = lane < 16 ? ((oj & 1) * 576 + oc) * 2 : 2304, vh3 = WS_H3 + (lane >> 2) * 4608 + mr * 64 + (lane & 3) * 16;
      PIN(b2v); PIN(vout); PIN(vh3);
      if (w == 4) {
        const int t = 256 + (lane & 31), hb = lane >> 5;
        f32x2 x0 = unpk(*(LAS unsigned*)(L + L_X + hb * 1152 + t * 4)), g0 = unpk(*(LAS unsigned*)(L + L_MOD + hb * 3584 + t * 4));
        unsigned px = (unsigned)(size_t)(L + L_X1) + hb * 2304 + t * 8;
        PIN(x0); PIN(g0); PIN(px);
        until(tl + TP2 / 10);
        const f32x4 s = gather<4, 4608>(rw, WS_H2 + hb * 4 * 4608 + t * 16, t4, lane, [] {});
        const auto c0 = __builtin_amdgcn_permlane32_swap(__float_as_uint(s[0]), __float_as_uint(s[1]), false, false);
        const auto c1 = __builtin_amdgcn_permlane32_swap(__float_as_uint(s[2]), __float_as_uint(s[3]), false, false);
        const f32x2 at = {__uint_as_float(c0[0]) + __uint_as_float(c0[1]), __uint_as_float(c1[0]) + __uint_as_float(c1[1])};
        *(LAS f32x2*)(size_t)px = rb2(x0 + rb2(g0 * rb2(at)));
        __builtin_amdgcn_s_sleep(4);
      }
      wait_flag2(L, F_W0);
#pragma unroll
      for (int t = 0; t < 8; ++t) { a2[2 * t] = trd2(p2 + 32 * t, p2 + 32 * t + 4 * 1152); a2[2 * t + 1] = trd2(p2 + 32 * t + 32 * 1152, p2 + 32 * t + 36 * 1152); }
      BAR();
      {
        f32x2 r = rb2(rb2(gemv2<9>(a, pb)) + b1v);
        r.x = r.x > 0.f ? r.x : 0.f; r.y = r.y > 0.f ? r.y : 0.f;
        r *= r;
        const unsigned hp_ = pk(r.x, r.y);
        *(LAS u16*)(size_t)pw_ = (u16)hp_;
        *(LAS u16*)(size_t)(pw_ + 128) = (u16)(hp_ >> 16);
      }
      BAR();
      {
        const v4u bq0 = *(LAS v4u*)(size_t)ph, bq1 = *(LAS v4u*)(size_t)(ph + 64);
        SB();
        asm volatile(
            "s_nop 1\n\t"
            "v_mfma_f32_16x16x32_bf16 v[176:179], %[b0], %[a0], 0\n\t"
            "v_mfma_f32_16x16x32_bf16 v[180:183], %[b0], %[a2], 0\n\t"
            "v_mfma_f32_16x16x32_bf16 v[176:179], %[b1], %[a1], v[176:179]\n\t"
            "v_mfma_f32_16x16x32_bf16 v[180:183], %[b1], %[a3], v[180:183]\n\t"
            "v_mfma_f32_16x16x32_bf16 v[184:187], %[b0], %[a4], 0\n\t"
            "v_mfma_f32_16x16x32_bf16 v[188:191], %[b0], %[a6], 0\n\t"
            "v_mfma_f32_16x16x32_bf16 v[184:187], %[b1], %[a5], v[184:187]\n\t"
            "v_mfma_f32_16x16x32_bf16 v[188:191], %[b1], %[a7], v[188:191]\n\t"
            "v_mfma_f32_16x16x32_bf16 v[192:195], %[b0], %[a8], 0\n\t"
            "v_mfma_f32_16x16x32_bf16 v[196:199], %[b0], %[a10], 0\n\t"
            "v_mfma_f32_16x16x32_bf16 v[192:195], %[b1], %[a9], v[192:195]\n\t"
            "v_mfma_f32_16x16x32_bf16 v[196:199], %[b1], %[a11], v[196:199]\n\t"
            "v_mfma_f32_16x16x32_bf16 v[200:203], %[b0], %[a12], 0\n\t"
            "v_and_b32_dpp v208, v176, %[mk] quad_perm:[0,1,2,3] row_mask:0x1 bank_mask:0xf\n\t"
            "v_mfma_f32_16x16x32_bf16 v[204:207], %[b0], %[a14], 0\n\t"
            "v_and_b32_dpp v209, v177, %[mk] quad_perm:[0,1,2,3] row_mask:0x1 bank_mask:0xf\n\t"
            "v_mfma_f32_16x16x32_bf16 v[200:203], %[b1], %[a13], v[200:203]\n\t"
            "v_and_b32_dpp v210, v180, %[mk] quad_perm:[0,1,2,3] row_mask:0x1 bank_mask:0xf\n\t"
            "v_mfma_f32_16x16x32_bf16 v[204:207], %[b1], %[a15], v[204:207]\n\t"
            "v_and_b32_dpp v211, v181, %[mk] quad_perm:[0,1,2,3] row_mask:0x1 bank_mask:0xf\n\t"
            "v_and_b32_dpp v208, v184, %[mk] quad_perm:[0,1,2,3] row_mask:0x2 bank_mask:0xf\n\t"
            "v_and_b32_dpp v209, v185, %[mk] quad_perm:[0,1,2,3] row_mask:0x2 bank_mask:0xf\n\t"
            "v_and_b32_dpp v210, v188, %[mk] quad_perm:[0,1,2,3] row_mask:0x2 bank_mask:0xf\n\t"
            "v_and_b32_dpp v211, v189, %[mk] quad_perm:[0,1,2,3] row_mask:0x2 bank_mask:0xf\n\t"
            "v_and_b32_dpp v208, v192, %[mk] quad_perm:[0,1,2,3] row_mask:0x4 bank_mask:0xf\n\t"
            "v_and_b32_dpp v209, v193, %[mk] quad_perm:[0,1,2,3] row_mask:0x4 bank_mask:0xf\n\t"
            "v_and_b32_dpp v210, v196, %[mk] quad_perm:[0,1,2,3] row_mask:0x4 bank_mask:0xf\n\t"
            "v_and_b32_dpp v211, v197, %[mk] quad_perm:[0,1,2,3] row_mask:0x4 bank_mask:0xf\n\t"
            "v_and_b32_dpp v208, v200, %[mk] quad_perm:[0,1,2,3] row_mask:0x8 bank_mask:0xf\n\t"
            "v_and_b32_dpp v209, v201, %[mk] quad_perm:[0,1,2,3] row_mask:0x8 bank_mask:0xf\n\t"
            "v_and_b32_dpp v210, v204, %[mk] quad_perm:[0,1,2,3] row_mask:0x8 bank_mask:0xf\n\t"
            "v_and_b32_dpp v211, v205, %[mk] quad_perm:[0,1,2,3] row_mask:0x8 bank_mask:0xf\n\t"
            "v_or_b32 v208, v208, v216\n\t"
            "v_or_b32 v209, v209, v217\n\t"
            "v_or_b32 v210, v210, v218\n\t"
            "v_or_b32 v211, v211, v219\n\t"
            "buffer_store_dwordx4 v[208:211], %[vo], %[rs], 0 offen sc1\n\t"
            "s_nop 1"
            :: [b0] "v"(bq0), [b1] "v"(bq1), [a0] "v"(a2[0]), [a1] "v"(a2[1]), [a2] "v"(a2[2]), [a3] "v"(a2[3]), [a4] "v"(a2[4]), [a5] "v"(a2[5]), [a6] "v"(a2[6]), [a7] "v"(a2[7]), [a8] "v"(a2[8]), [a9] "v"(a2[9]), [a10] "v"(a2[10]), [a11] "v"(a2[11]), [a12] "v"(a2[12]), [a13] "v"(a2[13]), [a14] "v"(a2[14]), [a15] "v"(a2[15]),
               [vo] "v"(v3), [rs] "s"(rw4), [mk] "v"(mkv), "{v[216:219]}"(tv)
            : "v176", "v177", "v178", "v179", "v180", "v181", "v182", "v183", "v184", "v185", "v186", "v187", "v188", "v189", "v190", "v191", "v192", "v193", "v194", "v195", "v196", "v197", "v198", "v199", "v200", "v201", "v202", "v203", "v204", "v205", "v206", "v207", "v208", "v209", "v210", "v211", "memory");
      }
      if (w == 5) {
        float xo = *(LAS float*)(L + L_X1 + ((oj & 1) * 576 + oc) * 4);
        PIN(xo);
        VM0();
        __builtin_amdgcn_s_sleep(1);
        until(tl + TP3 / 10);
        v4u g = ld128<AUX_SC1>(rw, vg, 0);
        SB();
        float gbv;
        unsigned long long gbad;
        auto gsel = [&] { const unsigned gp = (lane & 2) ? g.z : g.x; gbv = (lane & 1) ? bf_hi(gp) : bf_lo(gp); PIN(gbv); };
        f32x4 t = gather<5, 16 * 4608>(rw, vh3, t4, lane, [&] { gbad = __builtin_amdgcn_ballot_w64(((g.y ^ tag) | (g.w ^ tag)) != 0u); gsel(); });
        if (__builtin_expect(gbad != 0ull, 0)) {
          v4u g1[1];
          gatherg<1, 0, 1>(g1, rw, 0, vg, tag);
          g = g1[0];
          gsel();
        }
#pragma unroll
        for (int i = 0; i < 4; ++i) t[i] += dppf<ROR4>(t[i]);
#pragma unroll
        for (int i = 0; i < 4; ++i) t[i] += dppf<ROR8>(t[i]);
        float u = (oj & 2) ? ((oj & 1) ? t[3] : t[2]) : ((oj & 1) ? t[1] : t[0]);
        u = x32_sum(x16_sum(u));
        __builtin_amdgcn_raw_buffer_store_b16((u16)pk(xo + rb(gbv * rb(rb(u) + b2v)), 0.f), mkrs(out, 2304), vout, 0, AUX_SC1);
      } else if (w == 4 && mr == 0 && lane == 0) {
        __hip_atomic_store((GAS unsigned*)ws, tag, __ATOMIC_RELAXED, __HIP_MEMORY_SCOPE_AGENT);
      }
    } else if (is_gb) {
      wait_flag2(L, F_W0);
#pragma unroll
      for (int s = 0; s < 18; ++s) a[s] = trd2(pa + 32 * s * 128, pa + (32 * s + 4) * 128);
      const f32x4 d = gemv<18>(a, pact);
      st128(rw, n16 < 2 ? WS_MG + (n16 * 864 + (45 + ms) * 16 + hw * 4 + g4) * 16 : WS_END, v4u{pk(d[0], d[1]), tag, pk(d[2], d[3]), tag});
    }
  }
}
}

extern "C" void kernel_launch(void* const* d_in, const int* in_sizes, int n_in, void* d_out, int out_size, void* d_ws, size_t ws_size, hipStream_t stream) {
  using mk::u16;
  (void)hipFuncSetAttribute((const void*)mk::fused, hipFuncAttributeMaxDynamicSharedMemorySize, mk::L_END);
  mk::fused<<<256, mk::NTHR, mk::L_END, stream>>>((const u16*)d_in[0], (const u16*)d_in[1], (const u16*)d_in[2], (const u16*)d_in[3], (const u16*)d_in[4],
                                                  (const u16*)d_in[6], (const u16*)d_in[7], (const u16*)d_in[8], (const u16*)d_in[9], (const u16*)d_in[10],
                                                  (const u16*)d_in[11], (u16*)d_out, (unsigned char*)d_ws);
}
```
